```python
import torch

def _softmax(x, axis=-1):
    return torch.softmax(x, dim=axis)
def _gelu(x):
    return torch.nn.functional.gelu(x, approximate="tanh")
def _silu(x):
    return torch.nn.functional.silu(x)
def _sigmoid(x):
    return torch.sigmoid(x)
def _layernorm(x, axis=-1, eps=1e-5):
    m = x.mean(dim=axis, keepdim=True)
    v = x.var(dim=axis, keepdim=True, unbiased=False)
    return (x - m) / torch.sqrt(v + eps)
def _scatter_add(tgt, idx, src, axis=0):
    return tgt.clone().index_add_(axis, idx, src)
def _segment_sum(src, seg, n):
    out = torch.zeros((n,) + src.shape[1:], dtype=src.dtype)
    return out.index_add_(0, seg, src)
def _pad(x, pw):
    flat = tuple(v for pair in reversed(pw) for v in pair)
    return torch.nn.functional.pad(x, flat)

kernel_name = 'parallel_sum__sliding_block__mixer_token_mlp__prompt'

def setup_inputs():

    _w = (lambda a: (lambda t: t.to(torch.bfloat16) if t.is_floating_point() else t)(torch.as_tensor(a)))
    def _A_setup():
        B, S, D = 1, 2048, 1024
        return {
            "x": _w((lambda *s: torch.randn(*s, dtype=torch.bfloat16) * 0.05)(B, S, D)),
            "wqkv": _w((lambda *s: torch.randn(*s, dtype=torch.bfloat16) * 0.05)(D, 3 * D)),
            "wo": _w((lambda *s: torch.randn(*s, dtype=torch.bfloat16) * 0.05)(D, D)),
        }
    _A_in = _A_setup()

    def _B_setup():
        B, S, D, F = 1, 2048, 1024, 3584
        return {
            "x": _w((lambda *s: torch.randn(*s, dtype=torch.bfloat16) * 0.05)(B, S, D)),
            "w1": _w((lambda *s: torch.randn(*s, dtype=torch.bfloat16) * 3.2)(S, F) * (S ** -0.5)),
            "b1": _w((lambda *s: torch.randn(*s, dtype=torch.bfloat16) * 3.2)(F) * 0.02),
            "w2": _w((lambda *s: torch.randn(*s, dtype=torch.bfloat16) * 0.4)(F, S) * (F ** -0.5)),
            "b2": _w((lambda *s: torch.randn(*s, dtype=torch.bfloat16) * 0.4)(S) * 0.02),
        }
    _B_in = _B_setup()

    return {
        "A_x": _A_in["x"],
        "A_wqkv": _A_in["wqkv"],
        "A_wo": _A_in["wo"],
        "B_x": _B_in["x"],
        "B_w1": _B_in["w1"],
        "B_b1": _B_in["b1"],
        "B_w2": _B_in["w2"],
        "B_b2": _B_in["b2"],
    }

def reference(A_x, A_wqkv, A_wo, B_x, B_w1, B_b1, B_w2, B_b2):
    def _A_ref(x, wqkv, wo):
        B, S, D = x.shape
        H = 32; Dh = D // H; W = 256
        qkv = (lambda x, *d: x.permute(*d))((x @ wqkv).reshape(B, S, 3, H, Dh), 2, 0, 3, 1, 4)
        q, k, v = qkv[0], qkv[1], qkv[2]
        scores = torch.einsum("bhqd,bhkd->bhqk", q, k) * (Dh ** -0.5)
        idx = torch.arange(S)
        mask = (idx[:, None] - idx[None, :] >= 0) & (idx[:, None] - idx[None, :] < W)
        scores = torch.where(mask[None, None], scores, -1e9)
        p = _softmax(scores, axis=-1)
        o = torch.einsum("bhqk,bhkd->bhqd", p, v)
        return (lambda x, *d: x.permute(*d))(o, 0, 2, 1, 3).reshape(B, S, D) @ wo

    def _B_ref(x, w1, b1, w2, b2):
        B, S, D = x.shape
        h = _layernorm(x, axis=-1)
        u = torch.einsum("bsd,sf->bdf", h, w1) + b1
        u = _gelu(u)
        y = torch.einsum("bdf,fs->bds", u, w2) + b2
        return (lambda x, *d: x.permute(*d))(y, 0, 2, 1)

    x = A_x
    xn = _layernorm(x, axis=-1)
    return x + _A_ref(xn, A_wqkv, A_wo) + _B_ref(xn, B_w1, B_b1, B_w2, B_b2)
```

```cpp
#include <hip/hip_runtime.h>
#include <hip/hip_bf16.h>
#include <stdint.h>

typedef unsigned short u16;
typedef short bf16x8 __attribute__((ext_vector_type(8)));
typedef short s16x4 __attribute__((ext_vector_type(4)));
typedef float f32x4 __attribute__((ext_vector_type(4)));
typedef float f32x2 __attribute__((ext_vector_type(2)));
typedef __bf16 b16x2 __attribute__((ext_vector_type(2)));
typedef unsigned u32x4 __attribute__((ext_vector_type(4)));
typedef unsigned u32x2 __attribute__((ext_vector_type(2)));
#define LAS __attribute__((address_space(3)))
#define DI __device__ __forceinline__
typedef __amdgpu_buffer_rsrc_t srd_t;

constexpr int S_ = 2048, D_ = 1024, F_ = 3584, H_ = 32;
constexpr size_t WS_FLAG = 0;
constexpr size_t WS_XN = 65536;
constexpr size_t WS_H = WS_XN + (size_t)S_ * D_ * 2;
constexpr size_t WS_Q = WS_H + (size_t)S_ * D_ * 2;
constexpr size_t WS_K = WS_Q + (size_t)S_ * D_ * 2;
constexpr size_t WS_V = WS_K + (size_t)S_ * D_ * 2;
constexpr size_t WS_O = WS_V + (size_t)S_ * D_ * 2;
constexpr size_t WS_U = WS_O + (size_t)S_ * D_ * 2;
constexpr size_t WS_P = WS_U + (size_t)F_ * D_ * 2;
constexpr unsigned OOB = 0x80000000u;

DI float bf2f(unsigned b) { return __uint_as_float(b << 16); }
DI unsigned pk2(float a, float b) { f32x2 v = {a, b}; return __builtin_bit_cast(unsigned, __builtin_convertvector(v, b16x2)); }
DI float rbf(float a) { return __uint_as_float(pk2(a, 0.f) << 16); }
DI srd_t mk_srd(const void* p, unsigned bytes) { return __builtin_amdgcn_make_buffer_rsrc((void*)p, (short)0, (int)bytes, 0x00020000); }
DI void dma(unsigned lds_dst, unsigned voff, srd_t srd, unsigned soff) {
  asm volatile("s_mov_b32 m0, %0\n\ts_nop 0\n\tbuffer_load_dwordx4 %1, %2, %3 offen lds" ::"s"(lds_dst), "v"(voff), "s"(srd), "s"(soff) : "memory");
}
DI bf16x8 lds_frag(LAS const char* p) { return *(LAS const bf16x8*)p; }
DI bf16x8 lds_tr(LAS const char* p, int step) {
  s16x4 lo = __builtin_amdgcn_ds_read_tr16_b64_v4i16((LAS s16x4*)p);
  s16x4 hi = __builtin_amdgcn_ds_read_tr16_b64_v4i16((LAS s16x4*)(p + step));
  return __builtin_shufflevector(lo, hi, 0, 1, 2, 3, 4, 5, 6, 7);
}
#define MFMA(a, b, c) __builtin_amdgcn_mfma_f32_16x16x32_bf16((a), (b), (c), 0, 0, 0)
#define SBAR() __builtin_amdgcn_sched_barrier(0)
#define BAR() asm volatile("s_barrier" ::: "memory")
#define WAIT_BAR(n) asm volatile("s_waitcnt vmcnt(%0) lgkmcnt(0)\n\ts_barrier" ::"n"(n) : "memory")
DI u32x4 pack8(const f32x4& a, const f32x4& b) { return (u32x4){pk2(a[0], a[1]), pk2(a[2], a[3]), pk2(b[0], b[1]), pk2(b[2], b[3])}; }
DI float xsum(float v) {
#pragma unroll
  for (int o = 32; o >= 1; o >>= 1) v += __shfl_xor(v, o);
  return v;
}

constexpr int LN_PITCH = 2048 + 32;
DI void ln_row(float (&f)[16]) {
  float s = 0.f;
#pragma unroll
  for (int j = 0; j < 16; ++j) s += f[j];
  const float mean = xsum(s) * (1.0f / 1024.0f);
  float q = 0.f;
#pragma unroll
  for (int j = 0; j < 16; ++j) { const float d = f[j] - mean; q += d * d; }
  const float var = xsum(q) * (1.0f / 1024.0f);
  const float mb = rbf(mean), den = rbf(sqrtf(rbf(rbf(var) + 1e-5f)));
  const float inv = 1.0f / den;
#pragma unroll
  for (int j = 0; j < 16; ++j) f[j] = rbf(rbf(f[j] - mb) * inv);
}
__global__ __launch_bounds__(1024) void k_ln(const u16* __restrict__ x, char* __restrict__ ws) {
  __shared__ __attribute__((aligned(16))) char smem[2 * 16 * LN_PITCH];
  LAS char* lds = (LAS char*)smem;
  const int tid = threadIdx.x, w = tid >> 6, lane = tid & 63;
  const int sg = blockIdx.x >> 1, a = blockIdx.x & 1;
  if (tid == 0) *(unsigned*)(ws + WS_FLAG + blockIdx.x * 256) = 0u;
  const int row = 32 * sg + (w & 7) + 8 * a + 16 * (w >> 3);
  const u32x4 v0 = *(const u32x4*)(x + (size_t)row * D_ + lane * 8), v1 = *(const u32x4*)(x + (size_t)row * D_ + 512 + lane * 8);
  float f[16];
#pragma unroll
  for (int j = 0; j < 4; ++j) { f[2 * j] = bf2f(v0[j] & 0xffffu); f[2 * j + 1] = __uint_as_float(v0[j] & 0xffff0000u); f[8 + 2 * j] = bf2f(v1[j] & 0xffffu); f[9 + 2 * j] = __uint_as_float(v1[j] & 0xffff0000u); }
#pragma unroll
  for (int p = 0; p < 2; ++p) {
    ln_row(f);
    LAS char* r = lds + (p * 16 + w) * LN_PITCH + lane * 16;
    *(LAS u32x4*)r = (u32x4){pk2(f[0], f[1]), pk2(f[2], f[3]), pk2(f[4], f[5]), pk2(f[6], f[7])};
    *(LAS u32x4*)(r + 1024) = (u32x4){pk2(f[8], f[9]), pk2(f[10], f[11]), pk2(f[12], f[13]), pk2(f[14], f[15])};
  }
  __syncthreads();
#pragma unroll
  for (int it = 0; it < 2; ++it) {
    const int r8 = tid & 7, g = (tid >> 3) & 3, kg = tid >> 5;
    LAS const char* r = lds + (it * 8 + r8) * LN_PITCH + (32 * kg + 4 * g) * 2;
    const u32x2 lo = *(LAS const u32x2*)r, hi = *(LAS const u32x2*)(r + 32);
    *(u32x4*)(ws + WS_XN + ((size_t)((2 * sg + it) * 32 + kg) * 64 + g * 16 + 8 * a + r8) * 16) = (u32x4){lo[0], lo[1], hi[0], hi[1]};
  }
#pragma unroll
  for (int it = 0; it < 2; ++it) {
    const int G = (w * 2 + it) * 4 + (lane >> 4), df = G >> 1, gl = G & 1, q = (lane & 15) >> 2, p = lane & 3;
    LAS const char* r = lds + (16 + 4 * gl + q) * LN_PITCH + (32 * (df >> 1) + 8 * p + 4 * (df & 1)) * 2;
    const bf16x8 v = lds_tr(r, 8 * LN_PITCH);
    *(bf16x8*)(ws + WS_H + ((size_t)(df * 64 + sg) * 64 + (2 * a + gl) * 16 + (lane & 15)) * 16) = v;
  }
}

DI int kperm(int s, int o, int J, int NS) { int k = s + (((s & 3) == o) ? J : 0); return k >= NS ? k - NS : k; }
DI f32x2 gelu2(f32x2 v) {
  const f32x2 t = v * (v * v * (-0.044715f * 2.302208198f) + (-2.302208198f));
  f32x2 e; e.x = __builtin_amdgcn_exp2f(t.x); e.y = __builtin_amdgcn_exp2f(t.y);
  e = e + 1.0f;
  f32x2 r; r.x = __builtin_amdgcn_rcpf(e.x); r.y = __builtin_amdgcn_rcpf(e.y);
  return v * r;
}
DI f32x4 bias_gelu(f32x4 v, u32x2 b) {
  f32x2 lo = gelu2((f32x2){v[0] + bf2f(b[0] & 0xffffu), v[1] + __uint_as_float(b[0] & 0xffff0000u)});
  f32x2 hi = gelu2((f32x2){v[2] + bf2f(b[1] & 0xffffu), v[3] + __uint_as_float(b[1] & 0xffff0000u)});
  return (f32x4){lo.x, lo.y, hi.x, hi.y};
}

constexpr int B1_STAGE = 45056, B1_R = 3, B1_NS = 32, B1_J = 4;
template <int GRP>
DI void b1_tile(LAS char* lds, unsigned lds0, const u16* w1, const u16* b1, char* ws, int dt, int ft, int ko, int w, int lane) {
  constexpr int NW = GRP == 0 ? 4 : 3, CNT = GRP == 0 ? 4 : 7;
  const int wr = w >> 1, wc = w & 1, g = lane >> 4, q = (lane & 15) >> 2, p = lane & 3;
  const bool odd = ft & 1;
  const srd_t srd = GRP == 0 ? mk_srd(ws + WS_H, S_ * D_ * 2) : mk_srd(w1, S_ * F_ * 2);
  const unsigned voff = GRP == 0 ? lane * 16 : (lane >> 3) * (F_ * 2) + (((lane & 7) ^ (((lane >> 4) & 3) << 1)) * 16);
  const unsigned voffh = (lane >> 2) * (F_ * 2) + (((lane & 3) ^ (((lane >> 4) & 1) << 1)) * 16);
  const unsigned sbase = GRP == 0 ? (unsigned)(dt * 8 + 2 * w) * 65536u : (unsigned)(16 * w) * (F_ * 2) + 448u * ft + (odd ? 64u : 0u);
  const unsigned sbaseh = (unsigned)(16 * w) * (F_ * 2) + 448u * ft + (odd ? 0u : 384u);
  auto issue = [&](int s, int slot) {
    const int k = kperm(s, ko, B1_J, B1_NS);
    const unsigned dst = lds0 + slot * B1_STAGE;
    if (GRP == 0) {
      const unsigned so = s < B1_NS ? sbase + k * 2048u : OOB;
#pragma unroll
      for (int i = 0; i < 4; ++i) dma(dst + w * 4096 + i * 1024, voff, srd, so + (i >> 1) * 65536u + (i & 1) * 1024u);
    } else {
      const unsigned kb = s < B1_NS ? (unsigned)k * (64u * F_ * 2) : OOB;
#pragma unroll
      for (int i = 0; i < 6; ++i) dma(dst + 16384 + ((i >> 1) * 8 + 2 * w + (i & 1)) * 1024, voff, srd, sbase + kb + (i & 1) * (8u * F_ * 2) + (i >> 1) * 128u);
      dma(dst + 40960 + w * 1024, voffh, srd, sbaseh + kb);
    }
  };
  const int r = 4 * (g & 1) + q;
  int woff[NW];
#pragma unroll
  for (int i = 0; i < NW; ++i) {
    if (GRP == 0) woff[i] = 16384 + (wc * 8 + (g >> 1)) * 1024 + r * 128 + ((i ^ ((r >> 1) & 3)) * 32) + p * 8;
    else if (i < 2) woff[i] = 16384 + (16 + (g >> 1)) * 1024 + r * 128 + (((2 * wc + i) ^ ((r >> 1) & 3)) * 32) + p * 8;
    else woff[i] = 40960 + (4 * g + q) * 64 + ((wc ^ (g & 1)) * 32) + p * 8;
  }
  const int aoff = wr * 8192 + lane * 16;
  f32x4 acc[NW][4];
#pragma unroll
  for (int i = 0; i < NW; ++i)
#pragma unroll
    for (int j = 0; j < 4; ++j) acc[i][j] = (f32x4){0.f, 0.f, 0.f, 0.f};
#pragma unroll
  for (int s = 0; s < B1_R - 1; ++s) issue(s, s);
  WAIT_BAR(CNT * (B1_R - 2));
  if (GRP == 1) BAR();
  int rs = 0, wsl = B1_R - 1;
  for (int s = 0; s < B1_NS; ++s) {
    LAS const char* st = lds + rs * B1_STAGE;
    bf16x8 A[2][4], W[2][NW];
#pragma unroll
    for (int ks = 0; ks < 2; ++ks) {
#pragma unroll
      for (int i = 0; i < NW; ++i) W[ks][i] = (GRP == 1 && i == 2) ? lds_tr(st + woff[i] + ks * 2048, 1024) : lds_tr(st + woff[i] + ks * 4096, 2048);
#pragma unroll
      for (int j = 0; j < 4; ++j) A[ks][j] = lds_frag(st + aoff + j * 2048 + ks * 1024);
    }
    SBAR();
    issue(s + B1_R - 1, wsl);
    WAIT_BAR(CNT * (B1_R - 2));
    SBAR();
    __builtin_amdgcn_s_setprio(1);
#pragma unroll
    for (int ks = 0; ks < 2; ++ks)
#pragma unroll
      for (int i = 0; i < NW; ++i)
#pragma unroll
        for (int j = 0; j < 4; ++j) acc[i][j] = MFMA(W[ks][i], A[ks][j], acc[i][j]);
    __builtin_amdgcn_s_setprio(0);
    SBAR();
    BAR();
    wsl = rs; rs = rs == B1_R - 1 ? 0 : rs + 1;
  }
  if (GRP == 0) BAR();
  char* U = ws + WS_U;
  const int df0 = dt * 8 + wr * 4;
#pragma unroll
  for (int pr = 0; pr < 2; ++pr) {
    if (GRP == 1 && pr == 1) {
      const int fg = 7 * ft + (odd ? 0 : 6);
      const u32x2 b = *(const u32x2*)(b1 + fg * 32 + wc * 16 + 4 * g);
#pragma unroll
      for (int j = 0; j < 4; ++j) { const f32x4 v = bias_gelu(acc[2][j], b);
        *(u32x2*)(U + ((size_t)(df0 + j) * 112 + fg) * 1024 + lane * 16 + wc * 8) = (u32x2){pk2(v[0], v[1]), pk2(v[2], v[3])}; }
    } else {
      const int lp = GRP == 0 ? 2 * wc + pr : 4 + wc, fg = 7 * ft + lp + (odd ? 1 : 0);
      const u32x2 b0 = *(const u32x2*)(b1 + fg * 32 + 4 * g), bh = *(const u32x2*)(b1 + fg * 32 + 16 + 4 * g);
#pragma unroll
      for (int j = 0; j < 4; ++j)
        *(u32x4*)(U + ((size_t)(df0 + j) * 112 + fg) * 1024 + lane * 16) = pack8(bias_gelu(acc[2 * pr][j], b0), bias_gelu(acc[2 * pr + 1][j], bh));
    }
  }
}

constexpr int QK_STAGE = 28672, QK_R = 5, QK_NS = 32, QK_J = 8;
template <int GRP>
DI void qkv_tile(LAS char* lds, unsigned lds0, const u16* wqkv, char* ws, int st_, int hp, int ko, int w, int lane) {
  constexpr int CNT = GRP == 0 ? 4 : 3;
  const int wr = GRP * 2 + (w >> 1), wc = w & 1, g = lane >> 4, q = (lane & 15) >> 2, p = lane & 3;
  const srd_t srd = GRP == 0 ? mk_srd(ws + WS_XN, S_ * D_ * 2) : mk_srd(wqkv, D_ * 3 * D_ * 2);
  const unsigned voff = GRP == 0 ? lane * 16 : (lane >> 3) * 6144 + (((lane & 7) ^ (((lane >> 4) & 3) << 1)) * 16);
  const unsigned sbase = GRP == 0 ? (unsigned)(st_ * 16 + 4 * w) * 32768u : (unsigned)(8 * w) * 6144u + hp * 128u;
  auto issue = [&](int s, int slot) {
    const int k = kperm(s, ko, QK_J, QK_NS);
    const unsigned dst = lds0 + slot * QK_STAGE;
    if (GRP == 0) {
      const unsigned so = s < QK_NS ? sbase + k * 1024u : OOB;
#pragma unroll
      for (int i = 0; i < 4; ++i) dma(dst + (4 * w + i) * 1024, voff, srd, so + i * 32768u);
    } else {
      const unsigned so = s < QK_NS ? sbase + k * (32u * 6144u) : OOB;
#pragma unroll
      for (int i = 0; i < 3; ++i) dma(dst + 16384 + (i * 4 + w) * 1024, voff, srd, so + i * 2048u);
    }
  };
  const int r = 4 * (g & 1) + q;
  int woff[2];
#pragma unroll
  for (int i = 0; i < 2; ++i) woff[i] = 16384 + (g >> 1) * 1024 + r * 128 + (((2 * wc + i) ^ ((r >> 1) & 3)) * 32) + p * 8;
  const int aoff = wr * 4096 + lane * 16;
  f32x4 acc[6][4];
#pragma unroll
  for (int i = 0; i < 6; ++i)
#pragma unroll
    for (int j = 0; j < 4; ++j) acc[i][j] = (f32x4){0.f, 0.f, 0.f, 0.f};
#pragma unroll
  for (int s = 0; s < QK_R - 1; ++s) issue(s, s);
  WAIT_BAR(CNT * (QK_R - 2));
  if (GRP == 1) BAR();
  int rs = 0, wsl = QK_R - 1;
  for (int s = 0; s < QK_NS; ++s) {
    LAS const char* st = lds + rs * QK_STAGE;
    bf16x8 A[4], W[6];
#pragma unroll
    for (int i = 0; i < 6; ++i) W[i] = lds_tr(st + woff[i & 1] + (i >> 1) * 4096, 2048);
#pragma unroll
    for (int j = 0; j < 4; ++j) A[j] = lds_frag(st + aoff + j * 1024);
    SBAR();
    issue(s + QK_R - 1, wsl);
    WAIT_BAR(CNT * (QK_R - 2));
    SBAR();
    __builtin_amdgcn_s_setprio(1);
#pragma unroll
    for (int i = 0; i < 6; ++i)
#pragma unroll
      for (int j = 0; j < 4; ++j) acc[i][j] = i < 4 ? MFMA(W[i], A[j], acc[i][j]) : MFMA(A[j], W[i], acc[i][j]);
    __builtin_amdgcn_s_setprio(0);
    SBAR();
    BAR();
    wsl = rs; rs = rs == QK_R - 1 ? 0 : rs + 1;
  }
  if (GRP == 0) BAR();
  const int head = 2 * hp + wc, sf0 = st_ * 16 + wr * 4;
  const float qs = 0.17677669529663687f * 1.4426950408889634f;
#pragma unroll
  for (int j = 0; j < 4; ++j) {
    *(u32x4*)(ws + WS_Q + ((size_t)head * 128 + sf0 + j) * 1024 + lane * 16) = pack8(acc[0][j] * qs, acc[1][j] * qs);
    *(u32x4*)(ws + WS_K + ((size_t)head * 128 + sf0 + j) * 1024 + lane * 16) = pack8(acc[2][j], acc[3][j]);
    const int m = j >> 1, dh = j & 1;
    *(u32x4*)(ws + WS_V + (((size_t)head * 64 + (sf0 >> 1) + m) * 2 + dh) * 1024 + lane * 16) = pack8(acc[4 + dh][2 * m], acc[4 + dh][2 * m + 1]);
  }
}

constexpr int G1_LDS = QK_STAGE * QK_R > B1_STAGE * B1_R ? QK_STAGE * QK_R : B1_STAGE * B1_R;
__global__ __launch_bounds__(512, 2) void k_gemm1(const u16* __restrict__ wqkv, const u16* __restrict__ w1, const u16* __restrict__ b1, char* __restrict__ ws) {
  extern __shared__ __attribute__((aligned(16))) char smem[];
  LAS char* lds = (LAS char*)smem;
  const unsigned lds0 = (unsigned)(uintptr_t)lds;
  const int wid = __builtin_amdgcn_readfirstlane(threadIdx.x >> 6), lane = threadIdx.x & 63, w = wid & 3;
  const int x = blockIdx.x & 7, idx = blockIdx.x >> 3, t = idx & 15, ti = t >> 2, tj = t & 3, ko = (ti + tj) & 3;
  const int tm = (x & 1) * 4 + ti, tn = (x >> 1) * 4 + tj;
  if (idx < 16) {
    if (wid < 4) b1_tile<0>(lds, lds0, w1, b1, ws, tm, tn, ko, w, lane); else b1_tile<1>(lds, lds0, w1, b1, ws, tm, tn, ko, w, lane);
  } else {
    if (wid < 4) qkv_tile<0>(lds, lds0, wqkv, ws, tm, tn, ko, w, lane); else qkv_tile<1>(lds, lds0, wqkv, ws, tm, tn, ko, w, lane);
  }
}

DI float max_rows(float m) {
  m = fmaxf(m, __shfl_xor(m, 16)); return fmaxf(m, __shfl_xor(m, 32));
}
DI void attn_unit(LAS char* lds, unsigned lds0, char* ws, int head, int c, int wid, int lane) {
  const srd_t srk = mk_srd(ws + WS_K, S_ * D_ * 2), srv = mk_srd(ws + WS_V, S_ * D_ * 2);
  const unsigned sb = (unsigned)head * 131072u + (unsigned)(wid < 4 ? (c > 0 ? c - 1 : 0) : c) * 16384u + (wid & 3) * 4096u;
#pragma unroll
  for (int i = 0; i < 4; ++i) { dma(lds0 + wid * 4096 + i * 1024, lane * 16, srk, sb + i * 1024); dma(lds0 + 32768 + wid * 4096 + i * 1024, lane * 16, srv, sb + i * 1024); }
  const int sf = c * 16 + 2 * wid;
  bf16x8 Q[2];
#pragma unroll
  for (int b = 0; b < 2; ++b) Q[b] = *(const bf16x8*)(ws + WS_Q + ((size_t)head * 128 + sf + b) * 1024 + lane * 16);
  WAIT_BAR(0);
  LAS const char* kp = lds + wid * 2048 + lane * 16;
  LAS const char* vp = kp + 32768;
  const f32x4 z = {0.f, 0.f, 0.f, 0.f};
  f32x4 s[2][17];
#pragma unroll
  for (int kf = 0; kf < 18; ++kf) {
    const bf16x8 K = lds_frag(kp + kf * 1024);
    if (kf < 17) s[0][kf] = MFMA(K, Q[0], z);
    if (kf > 0) s[1][kf - 1] = MFMA(K, Q[1], z);
  }
  const int i = lane & 15, g4 = (lane >> 4) * 4;
  const float NEG = -1e30f;
#pragma unroll
  for (int b = 0; b < 2; ++b)
#pragma unroll
    for (int r = 0; r < 4; ++r) { if (!(g4 + r > i)) s[b][0][r] = NEG; if (g4 + r > i) s[b][16][r] = NEG; }
  if (c == 0) {
#pragma unroll
    for (int b = 0; b < 2; ++b)
#pragma unroll
      for (int n = 0; n < 16; ++n) if (n + b < 16 - 2 * wid) s[b][n] = (f32x4){NEG, NEG, NEG, NEG};
  }
  u32x4 P[2][9]; float inv[2];
#pragma unroll
  for (int b = 0; b < 2; ++b) {
    float m = NEG;
#pragma unroll
    for (int n = 0; n < 17; ++n) m = fmaxf(fmaxf(m, fmaxf(s[b][n][0], s[b][n][1])), fmaxf(s[b][n][2], s[b][n][3]));
    m = max_rows(m);
    float l = 0.f;
#pragma unroll
    for (int n = 0; n < 17; ++n) {
#pragma unroll
      for (int r = 0; r < 4; ++r) { s[b][n][r] = __builtin_amdgcn_exp2f(s[b][n][r] - m); l += s[b][n][r]; }
      const int kf = n + b;
      P[b][kf >> 1][(kf & 1) * 2] = pk2(s[b][n][0], s[b][n][1]); P[b][kf >> 1][(kf & 1) * 2 + 1] = pk2(s[b][n][2], s[b][n][3]);
    }
    if (b == 0) { P[0][8][2] = 0u; P[0][8][3] = 0u; } else { P[1][0][0] = 0u; P[1][0][1] = 0u; }
    l += __shfl_xor(l, 16); l += __shfl_xor(l, 32);
    inv[b] = 1.0f / l;
  }
  f32x4 o[2][2] = {{z, z}, {z, z}};
#pragma unroll
  for (int kg = 0; kg < 9; ++kg)
#pragma unroll
    for (int dh = 0; dh < 2; ++dh) {
      const bf16x8 V = lds_frag(vp + (kg * 2 + dh) * 1024);
#pragma unroll
      for (int b = 0; b < 2; ++b) o[b][dh] = MFMA(V, __builtin_bit_cast(bf16x8, P[b][kg]), o[b][dh]);
    }
#pragma unroll
  for (int b = 0; b < 2; ++b) *(u32x4*)(ws + WS_O + ((size_t)(sf + b) * 32 + head) * 1024 + lane * 16) = pack8(o[b][0] * inv[b], o[b][1] * inv[b]);
}
__global__ __launch_bounds__(512, 2) void k_attn(char* __restrict__ ws) {
  extern __shared__ __attribute__((aligned(16))) char smem[];
  LAS char* lds = (LAS char*)smem;
  const int wid = __builtin_amdgcn_readfirstlane(threadIdx.x >> 6), lane = threadIdx.x & 63;
  const int x = blockIdx.x & 7, idx = blockIdx.x >> 3;
  attn_unit(lds, (unsigned)(uintptr_t)lds, ws, x * 4 + (idx >> 3), idx & 7, wid, lane);
}

constexpr int G2_STAGE = 32768, G2_R = 4, G2_NA = 34, G2_N1 = 56 - G2_NA, G2_NB = G2_N1 + 16, G2_J = 4;
template <int GRP>
DI void gemm2_tile(LAS char* lds, unsigned lds0, const u16* x, const u16* wo, const u16* w2, const u16* b2, u16* out, char* ws, int st_, int dt, int role, int ko, int w, int lane) {
  const int wr = w >> 1, wc = w & 1, g = lane >> 4, q = (lane & 15) >> 2, p = lane & 3;
  const srd_t sru = mk_srd(ws + WS_U, F_ * D_ * 2), srw2 = mk_srd(w2, F_ * S_ * 2), sro = mk_srd(ws + WS_O, S_ * D_ * 2), srwo = mk_srd(wo, D_ * D_ * 2);
  const unsigned swz = ((lane & 7) ^ (((lane >> 4) & 3) << 1)) * 16;
  const unsigned vofa = lane * 16, vow2 = (lane >> 3) * 4096 + swz, vowo = (lane >> 3) * 2048 + swz;
  const int n1 = role ? G2_N1 : G2_NA, ns = role ? G2_NB : G2_NA, k0 = role ? G2_NA : 0;
  auto issue = [&](int s) {
    const unsigned dst = lds0 + (s & (G2_R - 1)) * G2_STAGE;
    if (s < n1) {
      const int s2 = s >> 1; int k = s + (((s2 & 3) == ko) ? 2 * G2_J : 0); k = k0 + (k >= n1 ? k - n1 : k);
      if ((G2_NA % 8) != 0 || (G2_N1 % 8) != 0) k = k0 + s;
      const unsigned sa = ((unsigned)(dt * 8 + 2 * w) * 112u + 2u * k) * 1024u, sw = (unsigned)(64 * k + 16 * w) * 4096u + st_ * 256u;
#pragma unroll
      for (int i = 0; i < 4; ++i) dma(dst + (4 * w + i) * 1024, vofa, sru, sa + (i >> 1) * (112u * 1024u) + (i & 1) * 1024u);
#pragma unroll
      for (int i = 0; i < 4; ++i) dma(dst + 16384 + ((i >> 1) * 8 + 2 * w + (i & 1)) * 1024, vow2, srw2, sw + (i & 1) * (8u * 4096u) + (i >> 1) * 128u);
    } else {
      const int k = s - n1;
      const unsigned sa = s < ns ? ((unsigned)(st_ * 8 + 2 * w) * 32u + 2u * k) * 1024u : OOB, sw = s < ns ? (unsigned)(64 * k + 16 * w) * 2048u + dt * 256u : OOB;
#pragma unroll
      for (int i = 0; i < 4; ++i) dma(dst + (4 * w + i) * 1024, vofa, sro, sa + (i >> 1) * (32u * 1024u) + (i & 1) * 1024u);
#pragma unroll
      for (int i = 0; i < 4; ++i) dma(dst + 16384 + ((i >> 1) * 8 + 2 * w + (i & 1)) * 1024, vowo, srwo, sw + (i & 1) * (8u * 2048u) + (i >> 1) * 128u);
    }
  };
  const int r = 4 * (g & 1) + q, sx = (r >> 1) & 3;
  f32x4 acc[4][4];
#pragma unroll
  for (int i = 0; i < 4; ++i)
#pragma unroll
    for (int j = 0; j < 4; ++j) acc[i][j] = (f32x4){0.f, 0.f, 0.f, 0.f};
  if (GRP == 1) { issue(0); issue(2); WAIT_BAR(8); BAR(); } else { issue(1); BAR(); }
  int s = GRP;
  {
    int woff[4];
#pragma unroll
    for (int i = 0; i < 4; ++i) woff[i] = 16384 + (wc * 8 + (g >> 1)) * 1024 + r * 128 + ((i ^ sx) * 32) + p * 8;
    const int aoff = wr * 8192 + lane * 16;
    for (; s < n1; s += 2) {
      LAS const char* st = lds + (s & (G2_R - 1)) * G2_STAGE;
      bf16x8 A[2][4], W[2][4];
#pragma unroll
      for (int ks = 0; ks < 2; ++ks) {
#pragma unroll
        for (int i = 0; i < 4; ++i) W[ks][i] = lds_tr(st + woff[i] + ks * 4096, 2048);
#pragma unroll
        for (int j = 0; j < 4; ++j) A[ks][j] = lds_frag(st + aoff + j * 2048 + ks * 1024);
      }
      SBAR();
      issue(s + G2_R - 1);
      WAIT_BAR(8);
      SBAR();
      __builtin_amdgcn_s_setprio(1);
#pragma unroll
      for (int ks = 0; ks < 2; ++ks)
#pragma unroll
        for (int i = 0; i < 4; ++i)
#pragma unroll
          for (int j = 0; j < 4; ++j) acc[i][j] = MFMA(A[ks][i], W[ks][j], acc[i][j]);
      __builtin_amdgcn_s_setprio(0);
      SBAR();
      BAR();
    }
  }
  {
    int woff[2];
#pragma unroll
    for (int m = 0; m < 2; ++m) woff[m] = 16384 + (wr * 8 + (g >> 1)) * 1024 + r * 128 + (((4 * m + p) ^ (sx << 1)) * 16);
    const int aoff = wc * 8192 + lane * 16;
    for (; s < ns; s += 2) {
      LAS const char* st = lds + (s & (G2_R - 1)) * G2_STAGE;
      bf16x8 A[2][4], W[2][4];
#pragma unroll
      for (int ks = 0; ks < 2; ++ks) {
#pragma unroll
        for (int i = 0; i < 4; ++i) W[ks][i] = lds_tr(st + woff[i >> 1] + (i & 1) * 8 + ks * 4096, 2048);
#pragma unroll
        for (int j = 0; j < 4; ++j) A[ks][j] = lds_frag(st + aoff + j * 2048 + ks * 1024);
      }
      SBAR();
      issue(s + G2_R - 1);
      WAIT_BAR(8);
      SBAR();
      __builtin_amdgcn_s_setprio(1);
#pragma unroll
      for (int ks = 0; ks < 2; ++ks)
#pragma unroll
        for (int i = 0; i < 4; ++i)
#pragma unroll
          for (int j = 0; j < 4; ++j) acc[i][j] = MFMA(W[ks][i], A[ks][j], acc[i][j]);
      __builtin_amdgcn_s_setprio(0);
      SBAR();
      BAR();
    }
  }
  if (GRP == 0) BAR();
  WAIT_BAR(0);
  {
    LAS char* xo = lds + (GRP * 4 + w) * 8192 + lane * 16;
    LAS const char* xi = lds + ((1 - GRP) * 4 + w) * 8192 + lane * 16;
#pragma unroll
    for (int i = 0; i < 4; ++i)
#pragma unroll
      for (int j = 0; j < 2; ++j) *(LAS f32x4*)(xo + (i * 2 + j) * 1024) = acc[i][2 * (1 - GRP) + j];
    WAIT_BAR(0);
#pragma unroll
    for (int i = 0; i < 4; ++i)
#pragma unroll
      for (int j = 0; j < 2; ++j) acc[i][2 * GRP + j] += *(LAS const f32x4*)(xi + (i * 2 + j) * 1024);
  }
  const int tile = st_ * 8 + dt;
  const srd_t srp = mk_srd(ws + WS_P + (size_t)tile * 65536, 65536);
  unsigned* flag = (unsigned*)(ws + WS_FLAG + tile * 256);
  const unsigned pofs = (GRP * 4 + w) * 8192 + lane * 16;
  if (role == 0) {
#pragma unroll
    for (int i = 0; i < 4; ++i)
#pragma unroll
      for (int j = 0; j < 2; ++j) __builtin_amdgcn_raw_buffer_store_b128(__builtin_bit_cast(u32x4, acc[i][2 * GRP + j]), srp, pofs + (i * 2 + j) * 1024, 0, 16);
    WAIT_BAR(0);
    if (GRP == 0 && w == 0 && lane == 0) __hip_atomic_store(flag, 1u, __ATOMIC_RELAXED, __HIP_MEMORY_SCOPE_AGENT);
  } else {
    u32x4 xv[2][2]; float bv[2];
#pragma unroll
    for (int j = 0; j < 2; ++j) {
      const int srow = st_ * 128 + wc * 64 + (2 * GRP + j) * 16 + (lane & 15);
      bv[j] = bf2f(b2[srow]);
#pragma unroll
      for (int m = 0; m < 2; ++m) xv[j][m] = *(const u32x4*)(x + (size_t)srow * D_ + dt * 128 + wr * 64 + 32 * m + 8 * g);
    }
    if (GRP == 0 && w == 0) {
      unsigned spins = 0;
      while (__builtin_amdgcn_readfirstlane(__hip_atomic_load(flag, __ATOMIC_RELAXED, __HIP_MEMORY_SCOPE_AGENT)) == 0u && ++spins < (1u << 24)) __builtin_amdgcn_s_sleep(1);
    }
    BAR();
#pragma unroll
    for (int i = 0; i < 4; ++i)
#pragma unroll
      for (int j = 0; j < 2; ++j) acc[i][2 * GRP + j] += __builtin_bit_cast(f32x4, __builtin_amdgcn_raw_buffer_load_b128(srp, pofs + (i * 2 + j) * 1024, 0, 16));
#pragma unroll
    for (int j = 0; j < 2; ++j) {
      const int srow = st_ * 128 + wc * 64 + (2 * GRP + j) * 16 + (lane & 15);
#pragma unroll
      for (int m = 0; m < 2; ++m) {
        const u32x4 xx = xv[j][m];
        f32x4 a = acc[2 * m][2 * GRP + j] + bv[j], b = acc[2 * m + 1][2 * GRP + j] + bv[j];
        a += (f32x4){bf2f(xx[0] & 0xffffu), __uint_as_float(xx[0] & 0xffff0000u), bf2f(xx[1] & 0xffffu), __uint_as_float(xx[1] & 0xffff0000u)};
        b += (f32x4){bf2f(xx[2] & 0xffffu), __uint_as_float(xx[2] & 0xffff0000u), bf2f(xx[3] & 0xffffu), __uint_as_float(xx[3] & 0xffff0000u)};
        *(u32x4*)(out + (size_t)srow * D_ + dt * 128 + wr * 64 + 32 * m + 8 * g) = pack8(a, b);
      }
    }
  }
}
__global__ __launch_bounds__(512, 2) void k_gemm2(const u16* __restrict__ x, const u16* __restrict__ wo, const u16* __restrict__ w2, const u16* __restrict__ b2, u16* __restrict__ out, char* __restrict__ ws) {
  extern __shared__ __attribute__((aligned(16))) char smem[];
  LAS char* lds = (LAS char*)smem;
  const unsigned lds0 = (unsigned)(uintptr_t)lds;
  const int wid = __builtin_amdgcn_readfirstlane(threadIdx.x >> 6), lane = threadIdx.x & 63, w = wid & 3;
  const int xc = blockIdx.x & 7, idx = blockIdx.x >> 3, role = idx >> 4, t = idx & 15, ti = t >> 2, tj = t & 3, ko = (ti + tj) & 3;
  const int st_ = (xc >> 1) * 4 + ti, dt = (xc & 1) * 4 + tj;
  if (wid < 4) gemm2_tile<0>(lds, lds0, x, wo, w2, b2, out, ws, st_, dt, role, ko, w, lane);
  else gemm2_tile<1>(lds, lds0, x, wo, w2, b2, out, ws, st_, dt, role, ko, w, lane);
}

extern "C" void kernel_launch(void* const* d_in, const int* in_sizes, int n_in, void* d_out, int out_size, void* d_ws, size_t ws_size, hipStream_t stream) {
  const u16* x = (const u16*)d_in[0];
  const u16* wqkv = (const u16*)d_in[1];
  const u16* wo = (const u16*)d_in[2];
  const u16* w1 = (const u16*)d_in[4];
  const u16* b1 = (const u16*)d_in[5];
  const u16* w2 = (const u16*)d_in[6];
  const u16* b2 = (const u16*)d_in[7];
  char* ws = (char*)d_ws;
  (void)hipFuncSetAttribute((const void*)k_gemm1, hipFuncAttributeMaxDynamicSharedMemorySize, G1_LDS);
  (void)hipFuncSetAttribute((const void*)k_gemm2, hipFuncAttributeMaxDynamicSharedMemorySize, G2_STAGE * G2_R);
  (void)hipFuncSetAttribute((const void*)k_attn, hipFuncAttributeMaxDynamicSharedMemorySize, 65536);
  k_ln<<<128, 1024, 0, stream>>>(x, ws);
  k_gemm1<<<256, 512, G1_LDS, stream>>>(wqkv, w1, b1, ws);
  k_attn<<<256, 512, 65536, stream>>>(ws);
  k_gemm2<<<256, 512, G2_STAGE * G2_R, stream>>>(x, wo, w2, b2, (u16*)d_out, ws);
}
```

```cpp
#include <hip/hip_runtime.h>
#include <hip/hip_bf16.h>
#include <stdint.h>

typedef unsigned short u16;
typedef short bf16x8 __attribute__((ext_vector_type(8)));
typedef short s16x4 __attribute__((ext_vector_type(4)));
typedef float f32x4 __attribute__((ext_vector_type(4)));
typedef float f32x2 __attribute__((ext_vector_type(2)));
typedef __bf16 b16x2 __attribute__((ext_vector_type(2)));
typedef unsigned u32x4 __attribute__((ext_vector_type(4)));
typedef unsigned u32x2 __attribute__((ext_vector_type(2)));
#define LAS __attribute__((address_space(3)))
#define DI __device__ __forceinline__
typedef __amdgpu_buffer_rsrc_t srd_t;

constexpr int S_ = 2048, D_ = 1024, F_ = 3584, H_ = 32;
constexpr size_t WS_FLAG = 0;
constexpr size_t WS_XN = 65536;
constexpr size_t WS_H = WS_XN + (size_t)S_ * D_ * 2;
constexpr size_t WS_Q = WS_H + (size_t)S_ * D_ * 2;
constexpr size_t WS_KV = WS_Q + (size_t)S_ * D_ * 2;
constexpr size_t WS_O = WS_KV + (size_t)S_ * D_ * 4;
constexpr size_t WS_U = WS_O + (size_t)S_ * D_ * 2;
constexpr size_t WS_P = WS_U + (size_t)F_ * D_ * 2;
constexpr unsigned OOB = 0x80000000u;

#define STAMP_DECL
#define STAMP_ARG
#define STAMP_PASS
#define STAMP(n) do {} while (0)
#define STAMP_FLUSH(kid) do {} while (0)
DI float bf2f(unsigned b) { return __uint_as_float(b << 16); }
DI unsigned pk2(float a, float b) { f32x2 v = {a, b}; return __builtin_bit_cast(unsigned, __builtin_convertvector(v, b16x2)); }
DI float rbf(float a) { return __uint_as_float(pk2(a, 0.f) << 16); }
DI srd_t mk_srd(const void* p, unsigned bytes) { return __builtin_amdgcn_make_buffer_rsrc((void*)p, (short)0, (int)bytes, 0x00020000); }
DI void dma(unsigned lds_dst, unsigned voff, srd_t srd, unsigned soff) {
  asm volatile("s_mov_b32 m0, %0\n\ts_nop 0\n\tbuffer_load_dwordx4 %1, %2, %3 offen lds" ::"s"(lds_dst), "v"(voff), "s"(srd), "s"(soff) : "memory");
}
DI void touch(unsigned lds_dst, unsigned voff, srd_t srd, unsigned soff) {
  asm volatile("s_nop 4\n\ts_mov_b32 m0, %0\n\ts_nop 0\n\tbuffer_load_dword %1, %2, %3 offen lds" ::"s"(lds_dst), "v"(voff), "s"(srd), "s"(soff) : "memory");
}
template <int N> DI void stouch(unsigned& sink, srd_t srd, unsigned off) {
#pragma unroll
  for (int j = 0; j < N; ++j) asm volatile("s_buffer_load_dword %0, %1, %2" : "+s"(sink) : "s"(srd), "s"(off + j * 64u) : "memory");
}
#define STAUX 16
DI void st16(srd_t srd, unsigned voff, unsigned soff, u32x4 v) { __builtin_amdgcn_raw_buffer_store_b128(v, srd, voff + soff, 0, STAUX); }
DI void st8(srd_t srd, unsigned voff, unsigned soff, u32x2 v) { __builtin_amdgcn_raw_buffer_store_b64(v, srd, voff + soff, 0, STAUX); }
DI bf16x8 lds_frag(LAS const char* p) { return *(LAS const bf16x8*)p; }
DI bf16x8 lds_tr(LAS const char* p, int step) {
  s16x4 lo = __builtin_amdgcn_ds_read_tr16_b64_v4i16((LAS s16x4*)p);
  s16x4 hi = __builtin_amdgcn_ds_read_tr16_b64_v4i16((LAS s16x4*)(p + step));
  return __builtin_shufflevector(lo, hi, 0, 1, 2, 3, 4, 5, 6, 7);
}
#define MFMA(a, b, c) __builtin_amdgcn_mfma_f32_16x16x32_bf16((a), (b), (c), 0, 0, 0)
#define SBAR() __builtin_amdgcn_sched_barrier(0)
#define BAR() asm volatile("s_barrier" ::: "memory")
#define LGKM0_BAR() asm volatile("s_waitcnt lgkmcnt(0)\n\ts_barrier" ::: "memory")
#define VM_BAR(n) asm volatile("s_waitcnt vmcnt(%0)\n\ts_barrier" ::"n"(n) : "memory")
#define WAIT_BAR(n) asm volatile("s_waitcnt vmcnt(%0) lgkmcnt(0)\n\ts_barrier" ::"n"(n) : "memory")
#define LGKM_BAR() asm volatile("s_waitcnt lgkmcnt(0)\n\ts_barrier" ::: "memory")
#define LBAR() BAR()
#define LINIT() do {} while (0)
DI u32x4 pack8(const f32x4& a, const f32x4& b) { return (u32x4){pk2(a[0], a[1]), pk2(a[2], a[3]), pk2(b[0], b[1]), pk2(b[2], b[3])}; }
template <int CTRL> DI float dpp_get(float v) { return __builtin_bit_cast(float, __builtin_amdgcn_update_dpp(0, __builtin_bit_cast(int, v), CTRL, 0xF, 0xF, true)); }
DI float xsum(float v) {
  v += dpp_get<0xB1>(v); v += dpp_get<0x4E>(v); v += dpp_get<0x141>(v); v += dpp_get<0x140>(v);
  auto a = __builtin_amdgcn_permlane16_swap(__float_as_uint(v), __float_as_uint(v), false, false); v = __uint_as_float(a[0]) + __uint_as_float(a[1]);
  auto b = __builtin_amdgcn_permlane32_swap(__float_as_uint(v), __float_as_uint(v), false, false); return __uint_as_float(b[0]) + __uint_as_float(b[1]);
}
DI f32x2 unpk(unsigned u) { return (f32x2){__uint_as_float(u << 16), __uint_as_float(u & 0xffff0000u)}; }

constexpr int LN_PITCH = 2048 + 16;
#define SPF_START 16
constexpr int PF_BLOCKS = 512, PF_PER = (SPF_START * 5 + 3) / 4;
struct Weights { const u16 *wqkv, *w1, *w2, *wo; };
DI void ln_row(unsigned (&u)[8]) {
  f32x2 f[8], s = {0.f, 0.f};
#pragma unroll
  for (int j = 0; j < 8; ++j) { f[j] = unpk(u[j]); s += f[j]; }
  const float mean = xsum(s.x + s.y) * (1.0f / 1024.0f);
  f32x2 q = {0.f, 0.f};
#pragma unroll
  for (int j = 0; j < 8; ++j) { const f32x2 d = f[j] - mean; q += d * d; }
  const float var = xsum(q.x + q.y) * (1.0f / 1024.0f);
  const float mb = rbf(mean), den = rbf(sqrtf(rbf(rbf(var) + 1e-5f)));
  const float inv = 1.0f / den;
#pragma unroll
  for (int j = 0; j < 8; ++j) { const f32x2 d = f[j] - mb; const f32x2 e = unpk(pk2(d.x, d.y)) * inv; u[j] = pk2(e.x, e.y); }
}
__global__ __launch_bounds__(512) void k_ln(const u16* __restrict__ x, Weights W, char* __restrict__ ws) {
  __shared__ __attribute__((aligned(16))) char smem[2 * 8 * LN_PITCH];
  LAS char* lds = (LAS char*)smem;
  const int tid = threadIdx.x, w = tid >> 6, lane = tid & 63, b = blockIdx.x;
  if (b >= 256) {
    const int v = __builtin_amdgcn_readfirstlane((b - 256) * 8 + w);
    const bool a = v < 2868;
    const srd_t srd = a ? mk_srd(W.w1, S_ * F_ * 2) : mk_srd(W.wqkv, D_ * 3 * D_ * 2);
    unsigned sink = 0;
    asm volatile("s_nop 4");
    stouch<PF_PER>(sink, srd, (unsigned)(a ? v : v - 2868) * (PF_PER * 64u));
    asm volatile("s_waitcnt lgkmcnt(0)" : "+s"(sink)::"memory");
    return;
  }
  STAMP_DECL STAMP(0);
  if (tid == 0) *(unsigned*)(ws + WS_FLAG + b * 256) = 0u;
  const int row = 8 * b + w;
  const u32x4 v0 = *(const u32x4*)(x + (size_t)row * D_ + lane * 8), v1 = *(const u32x4*)(x + (size_t)row * D_ + 512 + lane * 8);
  unsigned u[8] = {v0[0], v0[1], v0[2], v0[3], v1[0], v1[1], v1[2], v1[3]};
#pragma unroll
  for (int p = 0; p < 2; ++p) {
    ln_row(u);
    LAS char* r = lds + (p * 8 + w) * LN_PITCH + lane * 16;
    *(LAS u32x4*)r = (u32x4){u[0], u[1], u[2], u[3]};
    *(LAS u32x4*)(r + 1024) = (u32x4){u[4], u[5], u[6], u[7]};
  }
  STAMP(1);
  __syncthreads();
#pragma unroll
  for (int it = 0; it < 2; ++it) {
    const int r8 = tid & 7, cg = (tid >> 3) + 64 * it;
    const u32x4 v = *(LAS const u32x4*)(lds + r8 * LN_PITCH + cg * 16);
    st16(mk_srd(ws + WS_XN, S_ * D_ * 2), (cg * 16 + r8) * 16, (unsigned)(b >> 1) * 32768u + (b & 1) * 128u, v);
  }
#pragma unroll
  for (int it = 0; it < 2; ++it) {
    const int df = (w * 2 + it) * 4 + (lane >> 4), q = (lane & 15) >> 2, p = lane & 3;
    LAS const char* r = lds + (8 + q) * LN_PITCH + (32 * (df >> 1) + 8 * p + 4 * (df & 1)) * 2;
    const bf16x8 v = lds_tr(r, 4 * LN_PITCH);
    st16(mk_srd(ws + WS_H, S_ * D_ * 2), df * 65536 + (lane & 15) * 16, (unsigned)(b >> 2) * 1024u + (b & 3) * 256u, __builtin_bit_cast(u32x4, v));
  }
  STAMP(2); STAMP_FLUSH(0);
}

DI int kperm(int s, int o, int J, int NS) { int k = s + (((s & 3) == o) ? J : 0); return k >= NS ? k - NS : k; }
DI f32x2 gelu2(f32x2 v) {
  const f32x2 t = v * (v * v * (-0.044715f * 2.302208198f) + (-2.302208198f));
  f32x2 e; e.x = __builtin_amdgcn_exp2f(t.x); e.y = __builtin_amdgcn_exp2f(t.y);
  e = e + 1.0f;
  f32x2 r; r.x = __builtin_amdgcn_rcpf(e.x); r.y = __builtin_amdgcn_rcpf(e.y);
  return v * r;
}
DI f32x4 bias_gelu(f32x4 v, u32x2 b) {
  f32x2 lo = gelu2((f32x2){v[0] + bf2f(b[0] & 0xffffu), v[1] + __uint_as_float(b[0] & 0xffff0000u)});
  f32x2 hi = gelu2((f32x2){v[2] + bf2f(b[1] & 0xffffu), v[3] + __uint_as_float(b[1] & 0xffff0000u)});
  return (f32x4){lo.x, lo.y, hi.x, hi.y};
}

#define SPFN 1
#define SPF_DECL(blk) unsigned spf_sink = 0; const int spf_lw = (blk) * 4 + w; const bool spf_a = spf_lw < 717; \
  srd_t spf_srd = spf_a ? mk_srd(W.w1, S_ * F_ * 2) : mk_srd(W.wqkv, D_ * 3 * D_ * 2); \
  unsigned spf_step = spf_a ? 28u * 8192u : 12u * 8192u, spf_off = (spf_a ? spf_lw : spf_lw - 717) * 320u + SPF_START * spf_step
#define SPF(iv) do { if (SPFN) { if ((iv) == 64 - SPF_START) { spf_srd = mk_srd(W.w2, F_ * S_ * 2); spf_step = 512u * 320u; spf_off = (spf_lw & 511) * 320u + (spf_lw >> 9) * (G2_NA * 262144u); } \
    stouch<5>(spf_sink, spf_srd, spf_off); spf_off += spf_step; } } while (0)
#define SPF_END() asm volatile("s_waitcnt lgkmcnt(0)" : "+s"(spf_sink)::"memory")
#define B1J 0
#define QKJ 0
#define G2J 0
#define G2NA 22
constexpr int G2_STAGE = 32768, G2_R = 5, G2_NA = G2NA, G2_N1 = 56 - G2_NA, G2_NB = G2_N1 + 16, G2_J = G2J;
constexpr int B1_STAGE = 45056, B1_R = 3, B1_NS = 32, B1_J = B1J;
constexpr int G1_RING = 143360, G1_LDS = G1_RING + 256;
DI void b1_loader(unsigned lds0, const Weights& W, char* ws, int dt, int ft, int ko, int w, int lane, int j) {
  const u16* w1 = W.w1;
  const bool odd = ft & 1;
  const srd_t sra = mk_srd(ws + WS_H, S_ * D_ * 2), srw = mk_srd(w1, S_ * F_ * 2);
  const unsigned va = lane * 16, vw = (8 * (lane >> 5) + ((lane >> 3) & 3)) * (F_ * 2) + (((lane & 7) ^ (((lane >> 4) & 3) << 1)) * 16);
  const unsigned vh = (8 * (lane >> 4) + ((lane >> 2) & 3)) * (F_ * 2) + (((lane & 3) ^ (((lane >> 4) & 1) << 1)) * 16);
  const unsigned sba = (unsigned)(dt * 8 + 2 * w) * 65536u, sbw = (unsigned)(32 * (w >> 1) + 4 * (w & 1)) * (F_ * 2) + 448u * ft + (odd ? 64u : 0u);
  const unsigned sbh = (unsigned)(32 * (w >> 1) + 4 * (w & 1)) * (F_ * 2) + 448u * ft + (odd ? 0u : 384u);
  auto ldA = [&](int t, int slot) {
    const unsigned so = t < B1_NS ? sba + kperm(t, ko, B1_J, B1_NS) * 2048u : OOB, dst = lds0 + slot * B1_STAGE + w * 4096;
#pragma unroll
    for (int i = 0; i < 4; ++i) dma(dst + i * 1024, va, sra, so + (i >> 1) * 65536u + (i & 1) * 1024u);
  };
  auto ldW0 = [&](int t, int slot) {
    const unsigned so = t < B1_NS ? sbw + (unsigned)kperm(t, ko, B1_J, B1_NS) * (64u * F_ * 2) : OOB, dst = lds0 + slot * B1_STAGE + 16384 + w * 2048;
#pragma unroll
    for (int i = 0; i < 4; ++i) dma(dst + (i >> 1) * 8192 + (i & 1) * 1024, vw, srw, so + (i & 1) * (16u * F_ * 2) + (i >> 1) * 128u);
  };
  auto ldW1 = [&](int t, int slot) {
    const unsigned kb = t < B1_NS ? (unsigned)kperm(t, ko, B1_J, B1_NS) * (64u * F_ * 2) : OOB, dst = lds0 + slot * B1_STAGE;
#pragma unroll
    for (int i = 0; i < 2; ++i) dma(dst + 32768 + w * 2048 + i * 1024, vw, srw, sbw + kb + i * (16u * F_ * 2) + 256u);
    dma(dst + 40960 + w * 1024, vh, srw, sbh + kb);
  };
  ldW0(0, 0); ldA(0, 0); ldW0(1, 1); ldW1(0, 0); ldA(1, 1); ldW0(2, 2); ldW1(1, 1);
  VM_BAR(18);
  int s0 = 0, s1 = 1, s2 = 2;
  SPF_DECL(j);
  for (int s = 0; s < B1_NS; ++s) {
    ldA(s + 2, s2);
    SPF(2 * s);
    VM_BAR(15);
    ldW0(s + 3, s0); ldW1(s + 2, s2);
    SPF(2 * s + 1);
    VM_BAR(18);
    const int t = s0; s0 = s1; s1 = s2; s2 = t;
  }
  BAR();
  SPF_END();
}
template <int GRP>
DI void b1_tile(LAS char* lds, const u16* b1, char* ws, int dt, int ft, int w, int lane STAMP_ARG) {
  constexpr int NW = GRP == 0 ? 4 : 3;
  const int wr = w >> 1, wc = w & 1, g = lane >> 4, q = (lane & 15) >> 2, p = lane & 3;
  const bool odd = ft & 1;
  const int r = 4 * (g & 1) + q;
  int woff[NW];
#pragma unroll
  for (int i = 0; i < NW; ++i) {
    if (GRP == 0) woff[i] = 16384 + (wc * 8 + (g >> 1)) * 1024 + r * 128 + ((i ^ ((r >> 1) & 3)) * 32) + p * 8;
    else if (i < 2) woff[i] = 16384 + (16 + (g >> 1)) * 1024 + r * 128 + (((2 * wc + i) ^ ((r >> 1) & 3)) * 32) + p * 8;
    else woff[i] = 40960 + (4 * g + q) * 64 + ((wc ^ (g & 1)) * 32) + p * 8;
  }
  const int aoff = wr * 8192 + lane * 16;
  f32x4 acc[NW][4];
#pragma unroll
  for (int i = 0; i < NW; ++i)
#pragma unroll
    for (int j = 0; j < 4; ++j) acc[i][j] = (f32x4){0.f, 0.f, 0.f, 0.f};
  BAR();
  STAMP(1);
  if (GRP == 1) BAR();
  LINIT();
  int rs = 0;
  for (int s = 0; s < B1_NS; ++s) {
    LAS const char* st = lds + rs * B1_STAGE;
    bf16x8 A[2][4], W[2][NW];
#pragma unroll
    for (int ks = 0; ks < 2; ++ks) {
#pragma unroll
      for (int i = 0; i < NW; ++i) W[ks][i] = (GRP == 1 && i == 2) ? lds_tr(st + woff[i] + ks * 2048, 1024) : lds_tr(st + woff[i] + ks * 4096, 2048);
#pragma unroll
      for (int j = 0; j < 4; ++j) A[ks][j] = lds_frag(st + aoff + j * 2048 + ks * 1024);
    }
    LGKM_BAR();
    SBAR();
    __builtin_amdgcn_s_setprio(1);
#pragma unroll
    for (int ks = 0; ks < 2; ++ks)
#pragma unroll
      for (int i = 0; i < NW; ++i)
#pragma unroll
        for (int j = 0; j < 4; ++j) acc[i][j] = MFMA(W[ks][i], A[ks][j], acc[i][j]);
    __builtin_amdgcn_s_setprio(0);
    SBAR();
    LBAR();
    rs = rs == B1_R - 1 ? 0 : rs + 1;
  }
  STAMP(2);
  if (GRP == 0) BAR();
  const srd_t sru = mk_srd(ws + WS_U, F_ * D_ * 2);
  const int df0 = dt * 8 + wr * 4;
#pragma unroll
  for (int pr = 0; pr < 2; ++pr) {
    if (GRP == 1 && pr == 1) {
      const int fg = 7 * ft + (odd ? 0 : 6);
      const u32x2 b = *(const u32x2*)(b1 + fg * 32 + wc * 16 + 4 * g);
#pragma unroll
      for (int j = 0; j < 4; ++j) { const f32x4 v = bias_gelu(acc[2][j], b);
        st8(sru, lane * 16, (unsigned)((df0 + j) * 112 + fg) * 1024u + wc * 8, (u32x2){pk2(v[0], v[1]), pk2(v[2], v[3])}); }
    } else {
      const int lp = GRP == 0 ? 2 * wc + pr : 4 + wc, fg = 7 * ft + lp + (odd ? 1 : 0);
      const u32x2 b0 = *(const u32x2*)(b1 + fg * 32 + 4 * g), bh = *(const u32x2*)(b1 + fg * 32 + 16 + 4 * g);
#pragma unroll
      for (int j = 0; j < 4; ++j)
        st16(sru, lane * 16, (unsigned)((df0 + j) * 112 + fg) * 1024u, pack8(bias_gelu(acc[2 * pr][j], b0), bias_gelu(acc[2 * pr + 1][j], bh)));
    }
  }
}

constexpr int QK_STAGE = 28672, QK_R = 5, QK_NS = 32, QK_J = QKJ;
DI void qkv_loader(unsigned lds0, const Weights& W, char* ws, int st_, int hp, int ko, int w, int lane, int j) {
  const srd_t sra = mk_srd(ws + WS_XN, S_ * D_ * 2), srw = mk_srd(W.wqkv, D_ * 3 * D_ * 2);
  const unsigned va = lane * 16, vw = (8 * (lane >> 5) + ((lane >> 3) & 3)) * 6144 + (((lane & 7) ^ (((lane >> 4) & 3) << 1)) * 16);
  const unsigned sba = (unsigned)(st_ * 16 + 4 * w) * 32768u, sbw = (unsigned)(16 * (w & 1) + 4 * (w >> 1)) * 6144u + hp * 128u;
  auto ldA = [&](int t, int slot) {
    const unsigned so = t < QK_NS ? sba + kperm(t, ko, QK_J, QK_NS) * 1024u : OOB, dst = lds0 + slot * QK_STAGE + w * 4096;
#pragma unroll
    for (int i = 0; i < 4; ++i) dma(dst + i * 1024, va, sra, so + i * 32768u);
  };
  auto ldW = [&](int t, int slot) {
    const unsigned so = t < QK_NS ? sbw + kperm(t, ko, QK_J, QK_NS) * (32u * 6144u) : OOB, dst = lds0 + slot * QK_STAGE + 16384 + w * 1024;
#pragma unroll
    for (int i = 0; i < 3; ++i) dma(dst + i * 4096, vw, srw, so + i * 2048u);
  };
#pragma unroll
  for (int t = 0; t < QK_R - 1; ++t) { ldW(t, t); ldA(t, t); }
  VM_BAR(7 * (QK_R - 2));
  int wsl = QK_R - 1;
  SPF_DECL(128 + j);
  for (int s = 0; s < QK_NS; ++s) {
    ldW(s + QK_R - 1, wsl);
    SPF(2 * s);
    BAR();
    ldA(s + QK_R - 1, wsl);
    SPF(2 * s + 1);
    VM_BAR(7 * (QK_R - 2));
    wsl = wsl == QK_R - 1 ? 0 : wsl + 1;
  }
  BAR();
  SPF_END();
}
template <int GRP>
DI void qkv_tile(LAS char* lds, char* ws, int st_, int hp, int w, int lane STAMP_ARG) {
  const int wr = GRP * 2 + (w >> 1), wc = w & 1, g = lane >> 4, q = (lane & 15) >> 2, p = lane & 3;
  const int r = 4 * (g & 1) + q;
  int woff[2];
#pragma unroll
  for (int i = 0; i < 2; ++i) woff[i] = 16384 + (g >> 1) * 1024 + r * 128 + (((2 * wc + i) ^ ((r >> 1) & 3)) * 32) + p * 8;
  const int aoff = wr * 4096 + lane * 16;
  f32x4 acc[6][4];
#pragma unroll
  for (int i = 0; i < 6; ++i)
#pragma unroll
    for (int j = 0; j < 4; ++j) acc[i][j] = (f32x4){0.f, 0.f, 0.f, 0.f};
  BAR();
  STAMP(1);
  if (GRP == 1) BAR();
  LINIT();
  int rs = 0;
  for (int s = 0; s < QK_NS; ++s) {
    LAS const char* st = lds + rs * QK_STAGE;
    bf16x8 A[4], W[6];
#pragma unroll
    for (int i = 0; i < 6; ++i) W[i] = lds_tr(st + woff[i & 1] + (i >> 1) * 4096, 2048);
#pragma unroll
    for (int j = 0; j < 4; ++j) A[j] = lds_frag(st + aoff + j * 1024);
    LGKM_BAR();
    SBAR();
    __builtin_amdgcn_s_setprio(1);
#pragma unroll
    for (int i = 0; i < 6; ++i)
#pragma unroll
      for (int j = 0; j < 4; ++j) acc[i][j] = i < 4 ? MFMA(W[i], A[j], acc[i][j]) : MFMA(A[j], W[i], acc[i][j]);
    __builtin_amdgcn_s_setprio(0);
    SBAR();
    LBAR();
    rs = rs == QK_R - 1 ? 0 : rs + 1;
  }
  STAMP(2);
  if (GRP == 0) BAR();
  const int head = 2 * hp + wc, sf0 = st_ * 16 + wr * 4;
  const srd_t srq = mk_srd(ws + WS_Q, S_ * D_ * 6);
  const float qs = 0.17677669529663687f * 1.4426950408889634f;
#pragma unroll
  for (int j = 0; j < 4; ++j) {
    st16(srq, lane * 16, (unsigned)(head * 128 + sf0 + j) * 1024u, pack8(acc[0][j] * qs, acc[1][j] * qs));
    const int m = j >> 1, dh = j & 1;
    const unsigned kv = (unsigned)(S_ * D_ * 2) + (unsigned)(head * 64 + (sf0 >> 1) + m) * 4096u + dh * 1024u;
    st16(srq, lane * 16, kv, pack8(acc[2][j], acc[3][j]));
    st16(srq, lane * 16, kv + 2048u, pack8(acc[4 + dh][2 * m], acc[4 + dh][2 * m + 1]));
  }
}

static_assert(QK_STAGE * QK_R <= G1_RING && B1_STAGE * B1_R <= G1_RING, "ring");
__global__ __launch_bounds__(768, 3) void k_gemm1(Weights W, const u16* __restrict__ b1, char* __restrict__ ws) {
  extern __shared__ __attribute__((aligned(16))) char smem[];
  LAS char* lds = (LAS char*)smem;
  const unsigned lds0 = (unsigned)(uintptr_t)lds;
  const int wid = __builtin_amdgcn_readfirstlane(threadIdx.x >> 6), lane = threadIdx.x & 63, w = wid & 3;
  const int x = blockIdx.x & 7, idx = blockIdx.x >> 3, t = idx & 15, ti = t >> 2, tj = t & 3, ko = (ti + tj) & 3;
  const int tm = (x & 1) * 4 + ti, tn = (x >> 1) * 4 + tj;
  STAMP_DECL STAMP(0);
  if (idx < 16) {
    if (wid < 4) b1_tile<0>(lds, b1, ws, tm, tn, w, lane STAMP_PASS);
    else if (wid < 8) b1_tile<1>(lds, b1, ws, tm, tn, w, lane STAMP_PASS);
    else b1_loader(lds0, W, ws, tm, tn, ko, w, lane, (int)blockIdx.x);
  } else {
    if (wid < 4) qkv_tile<0>(lds, ws, tm, tn, w, lane STAMP_PASS);
    else if (wid < 8) qkv_tile<1>(lds, ws, tm, tn, w, lane STAMP_PASS);
    else qkv_loader(lds0, W, ws, tm, tn, ko, w, lane, (int)blockIdx.x - 128);
  }
  STAMP(3); STAMP_FLUSH(1);
}

DI float mx3(float a, float b, float c) { return __builtin_fmaxf(__builtin_fmaxf(a, b), c); }
DI float max_rows(float m) {
  auto a = __builtin_amdgcn_permlane16_swap(__float_as_uint(m), __float_as_uint(m), false, false); m = __builtin_fmaxf(__uint_as_float(a[0]), __uint_as_float(a[1]));
  auto b = __builtin_amdgcn_permlane32_swap(__float_as_uint(m), __float_as_uint(m), false, false); return __builtin_fmaxf(__uint_as_float(b[0]), __uint_as_float(b[1]));
}
DI f32x4 splat(float v) { return (f32x4){v, v, v, v}; }
template <bool FIRST>
DI void attn_unit(LAS const char* kv, const bf16x8 (&Q)[2], srd_t sro, unsigned ooff, int g0, int lane) {
  asm volatile("" : "+v"(lane), "+s"(g0));
  const int i = lane & 15, g4 = (lane >> 4) * 4;
  const float NEG = -1e30f;
  const f32x4 z = splat(0.f);
  f32x4 lo, hi;
#pragma unroll
  for (int r = 0; r < 4; ++r) { lo[r] = g4 + r > i ? 0.f : NEG; hi[r] = g4 + r > i ? NEG : 0.f; }
  constexpr int G0 = FIRST ? 1 : 0;
  auto live = [](int kg, int t) { return !((kg == 0 && t == 1) || (kg == 8 && t == 2)); };
  auto qk = [&](f32x4 (&sc)[4], const bf16x8 (&K)[2], int kg, const f32x4 (&c)[2], const f32x4 (&clo)[2], const f32x4 (&chi)[2]) {
#pragma unroll
    for (int t = 0; t < 4; ++t) {
      const int h = t >> 1, b = t & 1;
      if (!live(kg, t)) continue;
      const f32x4 cc = (FIRST && kg < 8) ? splat(kg >= g0 ? c[b][0] : NEG) : c[b];
      sc[t] = MFMA(K[h], Q[b], (kg == 0 && h == b) ? clo[b] : (kg == 8 && h == b) ? chi[b] : cc);
    }
  };
  auto ldk = [&](bf16x8 (&K)[2], int kg) { K[0] = lds_frag(kv + kg * 4096); K[1] = lds_frag(kv + kg * 4096 + 1024); };
  bf16x8 K[3][2]; f32x4 sc[2][4];
  float m[2] = {NEG, NEG};
  {
    const f32x4 c[2] = {z, z}, clo[2] = {lo, lo}, chi[2] = {hi, hi};
    ldk(K[G0 % 3], G0); ldk(K[(G0 + 1) % 3], G0 + 1);
#pragma unroll
    for (int kg = G0; kg < 10; ++kg) {
      if (kg + 2 < 9) ldk(K[(kg + 2) % 3], kg + 2);
      else ldk(K[(kg + 2) % 3], G0 + kg + 2 - 9);
      if (kg < 9) qk(sc[kg & 1], K[kg % 3], kg, c, clo, chi);
      if (kg > G0) {
#pragma unroll
        for (int t = 0; t < 4; ++t) if (live(kg - 1, t)) { const f32x4 v = sc[(kg - 1) & 1][t]; m[t & 1] = mx3(mx3(m[t & 1], v[0], v[1]), v[2], v[3]); }
      }
#pragma unroll
      for (int t = 0; t < 4; ++t) { __builtin_amdgcn_sched_group_barrier(0x8, 1, 0); __builtin_amdgcn_sched_group_barrier(0x2, 2, 0); }
      asm volatile("" : "+v"(m[0]), "+v"(m[1])::"memory");
      SBAR();
    }
  }
  const bf16x8 ones = {0x3f80, 0x3f80, 0x3f80, 0x3f80, 0x3f80, 0x3f80, 0x3f80, 0x3f80};
  f32x4 o[2][2] = {{z, z}, {z, z}}, l[2] = {z, z};
  {
    f32x4 c[2], clo[2], chi[2];
#pragma unroll
    for (int b = 0; b < 2; ++b) { c[b] = splat(-max_rows(m[b])); clo[b] = c[b] + lo; chi[b] = c[b] + hi; }
    u32x4 P[2][2]; bf16x8 V[2][2];
#pragma unroll
    for (int kg = G0; kg < 11; ++kg) {
      if (kg < 9) qk(sc[kg & 1], K[(kg - G0) % 3], kg, c, clo, chi);
      if (kg + 3 < 9) ldk(K[(kg - G0) % 3], kg + 3);
      if (kg - 1 >= G0 && kg - 1 < 9) {
        V[(kg - 1) & 1][0] = lds_frag(kv + (kg - 1) * 4096 + 2048); V[(kg - 1) & 1][1] = lds_frag(kv + (kg - 1) * 4096 + 3072);
#pragma unroll
        for (int t = 0; t < 4; ++t) {
          const f32x4 v = sc[(kg - 1) & 1][t]; const bool lv = live(kg - 1, t);
          P[(kg - 1) & 1][t & 1][(t >> 1) * 2] = lv ? pk2(__builtin_amdgcn_exp2f(v[0]), __builtin_amdgcn_exp2f(v[1])) : 0u;
          P[(kg - 1) & 1][t & 1][(t >> 1) * 2 + 1] = lv ? pk2(__builtin_amdgcn_exp2f(v[2]), __builtin_amdgcn_exp2f(v[3])) : 0u;
        }
      }
      if (kg - 2 >= G0) {
#pragma unroll
        for (int b = 0; b < 2; ++b) {
          const bf16x8 pb = __builtin_bit_cast(bf16x8, P[kg & 1][b]);
          o[b][0] = MFMA(V[kg & 1][0], pb, o[b][0]); o[b][1] = MFMA(V[kg & 1][1], pb, o[b][1]); l[b] = MFMA(ones, pb, l[b]);
        }
      }
#pragma unroll
      for (int t = 0; t < 8; ++t) { __builtin_amdgcn_sched_group_barrier(0x400, 2, 0); __builtin_amdgcn_sched_group_barrier(0x8, 1, 0); __builtin_amdgcn_sched_group_barrier(0x2, 1, 0); }
      if (kg - 1 >= G0 && kg - 1 < 9) asm volatile("" : "+v"(P[(kg - 1) & 1][0]), "+v"(P[(kg - 1) & 1][1])::"memory");
      SBAR();
    }
  }
#pragma unroll
  for (int b = 0; b < 2; ++b) {
    const float inv = 1.0f / l[b][0];
    __builtin_amdgcn_raw_buffer_store_b128(pack8(o[b][0] * inv, o[b][1] * inv), sro, lane * 16 + ooff + b * 32768u, 0, 16);
  }
}

constexpr int SPF2 = 7;
DI int kperm2(int s, int o, int n) { const int n4 = n & ~3; int k = s + ((((s & 3) == o) && s < n4) ? G2_J : 0); return (k >= n4 && s < n4) ? k - n4 : k; }
template <int ROLE>
DI void gemm2_loader(unsigned lds0, const Weights& W, char* ws, int st_, int dt, int ko, int w, int lane, int j) {
  constexpr int P = ROLE ? 4 : 3, N1 = ROLE ? G2_N1 : G2_NA, NS = ROLE ? G2_NB : G2_NA, K0 = ROLE ? G2_NA : 0;
  const srd_t sru = mk_srd(ws + WS_U, F_ * D_ * 2), srw2 = mk_srd(W.w2, F_ * S_ * 2), sro = mk_srd(ws + WS_O, S_ * D_ * 2), srwo = mk_srd(W.wo, D_ * D_ * 2);
  const unsigned swz = ((lane & 7) ^ (((lane >> 4) & 3) << 1)) * 16;
  const unsigned vofa = lane * 16, vow2 = (lane >> 3) * 4096 + swz, vowo = (lane >> 3) * 2048 + swz;
  auto issue = [&](int s, int slot) {
    const unsigned dst = lds0 + slot * G2_STAGE;
    if (!ROLE || s < N1) {
      const int k = K0 + kperm2(s, ko, N1);
      const unsigned sa = ((unsigned)(dt * 8 + 2 * w) * 112u + 2u * k) * 1024u, sw = (unsigned)(64 * k + 16 * w) * 4096u + st_ * 256u;
#pragma unroll
      for (int i = 0; i < 4; ++i) dma(dst + 16384 + ((i >> 1) * 8 + 2 * w + (i & 1)) * 1024, vow2, srw2, sw + (i & 1) * (8u * 4096u) + (i >> 1) * 128u);
#pragma unroll
      for (int i = 0; i < 4; ++i) dma(dst + (4 * w + i) * 1024, vofa, sru, sa + (i >> 1) * (112u * 1024u) + (i & 1) * 1024u);
    } else {
      const int k = kperm2(s - N1, ko, 16);
      const unsigned sa = ((unsigned)(st_ * 8 + 2 * w) * 32u + 2u * k) * 1024u, sw = (unsigned)(64 * k + 16 * w) * 2048u + dt * 256u;
#pragma unroll
      for (int i = 0; i < 4; ++i) dma(dst + 16384 + ((i >> 1) * 8 + 2 * w + (i & 1)) * 1024, vowo, srwo, sw + (i & 1) * (8u * 2048u) + (i >> 1) * 128u);
#pragma unroll
      for (int i = 0; i < 4; ++i) dma(dst + (4 * w + i) * 1024, vofa, sro, sa + (i >> 1) * (32u * 1024u) + (i & 1) * 1024u);
    }
  };
  if (ROLE == 0) {
    const srd_t skv = mk_srd(ws + WS_KV, S_ * D_ * 4);
    const int head = j >> 2, sc = j & 3;
    auto ldkv = [&](int c) {
      const int ch = 2 * sc - 1 + c;
      const unsigned so = (unsigned)head * 262144u + (unsigned)(ch < 0 ? 0 : ch) * 32768u + w * 8192u;
#pragma unroll
      for (int i = 0; i < 8; ++i) dma(lds0 + (2 + c) * G2_STAGE + w * 8192 + i * 1024, vofa, skv, so + i * 1024u);
    };
    ldkv(0); ldkv(1);
    VM_BAR(0);
    ldkv(2); issue(0, 0); issue(1, 1);
    VM_BAR(16);
    issue(2, 2);
    VM_BAR(16);
  } else {
    issue(0, 0); issue(1, 1);
    VM_BAR(8);
    issue(2, 2); issue(3, 3);
  }
  unsigned sink = 0;
  const unsigned pw = (unsigned)(j * 4 + w) * (SPF2 * 64u), pstep = 512u * SPF2 * 64u;
  srd_t psrd = mk_srd(W.w2, ROLE ? 56u * 262144u : G2_NA * 262144u);
  unsigned poff = pw + (ROLE ? G2_NA * 262144u : 0u) + SPF_START * 512u * 320u;
  constexpr int PSW = (G2_N1 * 262144 - SPF_START * 512 * 320 + 512 * SPF2 * 64 - 1) / (512 * SPF2 * 64);
  int slot = P;
  for (int s = 0; s < NS - P; ++s) {
    issue(s + P, slot);
    slot = slot == G2_R - 1 ? 0 : slot + 1;
    if (ROLE && s == (PSW > 0 ? PSW : 0)) { psrd = srwo; poff = pw; }
    if (SPFN) stouch<SPF2>(sink, psrd, poff);
    poff += pstep;
    VM_BAR(8 * (P - 1));
  }
  asm volatile("s_waitcnt lgkmcnt(0)" : "+s"(sink)::"memory");
  if (P == 4) VM_BAR(16);
  VM_BAR(8);
  VM_BAR(0);
}

DI void attn_phase(LAS char* lds, char* ws, int wid, int lane, int j STAMP_ARG) {
  const int head = j >> 2, sc = j & 3, sf = sc * 32 + 2 * wid;
  bf16x8 Q[2], Qn[2];
#pragma unroll
  for (int b = 0; b < 2; ++b) {
    Q[b] = *(const bf16x8*)(ws + WS_Q + ((size_t)head * 128 + sf + b) * 1024 + lane * 16);
    Qn[b] = *(const bf16x8*)(ws + WS_Q + ((size_t)head * 128 + sf + 16 + b) * 1024 + lane * 16);
  }
  const srd_t sro = mk_srd(ws + WS_O, S_ * D_ * 2);
  LAS const char* kv = lds + 2 * G2_STAGE + wid * 4096 + lane * 16;
  unsigned ooff = (unsigned)(sf * 32 + head) * 1024u;
  asm volatile("s_waitcnt vmcnt(0)" : "+v"(Q[0]), "+v"(Q[1]), "+v"(Qn[0]), "+v"(Qn[1])::"memory");
#pragma nounroll
  for (int u = 0; u < 2; ++u) {
    LGKM0_BAR();
    if (u == 0) STAMP(1);
    if (sc == 0 && u == 0) attn_unit<true>(kv, Q, sro, ooff, 8 - wid, lane); else attn_unit<false>(kv, Q, sro, ooff, 0, lane);
    Q[0] = Qn[0]; Q[1] = Qn[1]; kv += G2_STAGE; ooff += 16u * 32768u;
  }
  WAIT_BAR(0);
  if (wid == 0 && lane == 0) __hip_atomic_fetch_add((unsigned*)(ws + WS_FLAG + (128 + sc) * 256), 1u, __ATOMIC_RELAXED, __HIP_MEMORY_SCOPE_AGENT);
  STAMP(2);
}
struct F { static constexpr bool value = false; };
struct T { static constexpr bool value = true; };
template <int GRP, int ROLE>
DI void gemm2_tile(LAS char* lds, const u16* x, const u16* b2, u16* out, char* ws, int st_, int dt, int w, int lane STAMP_ARG) {
  constexpr int N1 = ROLE ? G2_N1 : G2_NA, NS = ROLE ? G2_NB : G2_NA;
  const int wr = w >> 1, wc = w & 1, g = lane >> 4, q = (lane & 15) >> 2, p = lane & 3;
  unsigned* actr = (unsigned*)(ws + WS_FLAG + (128 + (st_ >> 2)) * 256);
  if (ROLE == 1) { BAR(); STAMP(1); }
  const int r = 4 * (g & 1) + q, sx = (r >> 1) & 3;
  f32x4 acc[4][4];
#pragma unroll
  for (int i = 0; i < 4; ++i)
#pragma unroll
    for (int jj = 0; jj < 4; ++jj) acc[i][jj] = splat(0.f);
  if (GRP == 1) BAR();
  LINIT();
  int s = GRP, slot = GRP;
  int woff[4], aoff;
  auto step = [&](auto part) {
    constexpr bool P2 = decltype(part)::value;
    LAS const char* st = lds + slot * G2_STAGE;
    bf16x8 A[2][4], W[2][4];
#pragma unroll
    for (int f = 0; f < 8; ++f) {
      W[f >> 2][f & 3] = lds_tr(st + woff[f & 3] + (f >> 2) * 4096, 2048);
      A[f >> 2][f & 3] = lds_frag(st + aoff + (f & 3) * 2048 + (f >> 2) * 1024);
    }
    LGKM_BAR();
    SBAR();
    __builtin_amdgcn_s_setprio(1);
#pragma unroll
    for (int ks = 0; ks < 2; ++ks)
#pragma unroll
      for (int i = 0; i < 4; ++i)
#pragma unroll
        for (int jj = 0; jj < 4; ++jj) acc[i][jj] = P2 ? MFMA(W[ks][i], A[ks][jj], acc[i][jj]) : MFMA(A[ks][i], W[ks][jj], acc[i][jj]);
    __builtin_amdgcn_s_setprio(0);
    SBAR();
    LBAR();
    s += 2; slot += 2; if (slot >= G2_R) slot -= G2_R;
  };
#pragma unroll
  for (int i = 0; i < 4; ++i) woff[i] = 16384 + (wc * 8 + (g >> 1)) * 1024 + r * 128 + ((i ^ sx) * 32) + p * 8;
  aoff = wr * 8192 + lane * 16;
  if (ROLE == 1 && GRP == 0) {
    _Pragma("nounroll") while (s < N1 - 14) step(F{});
    unsigned seen = 0;
    if (w == 0) seen = __hip_atomic_load(actr, __ATOMIC_RELAXED, __HIP_MEMORY_SCOPE_AGENT);
    _Pragma("nounroll") while (s < N1 - 10) step(F{});
    asm volatile("" : "+v"(seen));
    if (w == 0) {
      unsigned spins = 0;
      while (__builtin_amdgcn_readfirstlane(seen) < 32u && ++spins < (1u << 22)) { __builtin_amdgcn_s_sleep(2); seen = __hip_atomic_load(actr, __ATOMIC_RELAXED, __HIP_MEMORY_SCOPE_AGENT); }
      __builtin_amdgcn_fence(__ATOMIC_ACQUIRE, "agent");
    }
    _Pragma("nounroll") while (s < N1 - 6) step(F{});
    asm volatile("s_waitcnt vmcnt(0)" ::: "memory");
  }
  _Pragma("nounroll") while (s < N1) step(F{});
  if (ROLE == 1) {
    STAMP(2);
#pragma unroll
    for (int i = 0; i < 4; ++i) woff[i] = 16384 + (wr * 8 + (g >> 1)) * 1024 + r * 128 + (((4 * (i >> 1) + p) ^ (sx << 1)) * 16) + (i & 1) * 8;
    aoff = wc * 8192 + lane * 16;
    _Pragma("nounroll") while (s < NS) step(T{});
  }
  STAMP(3);
  const int tile = st_ * 8 + dt;
  const srd_t srp = mk_srd(ws + WS_P + (size_t)tile * 65536, 65536);
  unsigned* flag = (unsigned*)(ws + WS_FLAG + tile * 256);
  const unsigned pofs = (GRP * 4 + w) * 8192 + lane * 16;
  LAS char* xo = lds + (GRP * 4 + w) * 8192 + lane * 16;
  LAS const char* xi = lds + ((1 - GRP) * 4 + w) * 8192 + lane * 16;
  const int srow = st_ * 128 + wc * 64 + GRP * 32 + (lane & 15), dcol = dt * 128 + wr * 64 + 8 * g;
  u32x4 xv[2][2], pv[4][2]; float bv[2];
  auto fetch = [&]() {
    if (ROLE == 0) {
#pragma unroll
      for (int jj = 0; jj < 2; ++jj) {
        bv[jj] = bf2f(b2[srow + jj * 16]);
#pragma unroll
        for (int m = 0; m < 2; ++m) xv[jj][m] = *(const u32x4*)(x + (size_t)(srow + jj * 16) * D_ + dcol + 32 * m);
      }
    } else {
#pragma unroll
      for (int i = 0; i < 4; ++i)
#pragma unroll
        for (int jj = 0; jj < 2; ++jj) pv[i][jj] = __builtin_amdgcn_raw_buffer_load_b128(srp, pofs + (i * 2 + jj) * 1024, 0, 16);
    }
  };
  auto send = [&]() {
#pragma unroll
    for (int i = 0; i < 4; ++i)
#pragma unroll
      for (int jj = 0; jj < 2; ++jj) *(LAS f32x4*)(xo + (i * 2 + jj) * 1024) = acc[i][2 * (1 - GRP) + jj];
  };
  auto recv = [&]() {
#pragma unroll
    for (int i = 0; i < 4; ++i)
#pragma unroll
      for (int jj = 0; jj < 2; ++jj) acc[i][2 * GRP + jj] += *(LAS const f32x4*)(xi + (i * 2 + jj) * 1024);
  };
  if (GRP == 0) {
    send();
    if (ROLE == 1) {
      unsigned spins = 0;
      while (__builtin_amdgcn_readfirstlane(__hip_atomic_load(flag, __ATOMIC_RELAXED, __HIP_MEMORY_SCOPE_AGENT)) == 0u && ++spins < (1u << 22)) __builtin_amdgcn_s_sleep(1);
      __builtin_amdgcn_fence(__ATOMIC_ACQUIRE, "wavefront");
    }
    fetch();
    LGKM0_BAR();
    BAR();
    recv();
  } else {
    fetch();
    send();
    recv();
    LGKM0_BAR();
  }
  STAMP(4);
  if (ROLE == 0) {
#pragma unroll
    for (int jj = 0; jj < 2; ++jj)
#pragma unroll
      for (int m = 0; m < 2; ++m) {
        const u32x4 xx = xv[jj][m];
        const f32x2 x0 = unpk(xx[0]), x1 = unpk(xx[1]), x2 = unpk(xx[2]), x3 = unpk(xx[3]);
        const f32x4 a = acc[2 * m][2 * GRP + jj] + bv[jj] + (f32x4){x0.x, x0.y, x1.x, x1.y}, b = acc[2 * m + 1][2 * GRP + jj] + bv[jj] + (f32x4){x2.x, x2.y, x3.x, x3.y};
        __builtin_amdgcn_raw_buffer_store_b128(__builtin_bit_cast(u32x4, a), srp, pofs + (4 * m + jj) * 1024, 0, 16);
        __builtin_amdgcn_raw_buffer_store_b128(__builtin_bit_cast(u32x4, b), srp, pofs + (4 * m + 2 + jj) * 1024, 0, 16);
      }
    WAIT_BAR(0);
    if (GRP == 0 && w == 0 && lane == 0) __hip_atomic_store(flag, 1u, __ATOMIC_RELAXED, __HIP_MEMORY_SCOPE_AGENT);
  } else {
#pragma unroll
    for (int jj = 0; jj < 2; ++jj)
#pragma unroll
      for (int m = 0; m < 2; ++m)
        *(u32x4*)(out + (size_t)(srow + jj * 16) * D_ + dcol + 32 * m) =
            pack8(acc[2 * m][2 * GRP + jj] + __builtin_bit_cast(f32x4, pv[2 * m][jj]), acc[2 * m + 1][2 * GRP + jj] + __builtin_bit_cast(f32x4, pv[2 * m + 1][jj]));
  }
}
__global__ __launch_bounds__(768, 3) void k_gemm2(const u16* __restrict__ x, Weights W, const u16* __restrict__ b2, u16* __restrict__ out, char* __restrict__ ws) {
  extern __shared__ __attribute__((aligned(16))) char smem[];
  LAS char* lds = (LAS char*)smem;
  const unsigned lds0 = (unsigned)(uintptr_t)lds;
  const int wid = __builtin_amdgcn_readfirstlane(threadIdx.x >> 6), lane = threadIdx.x & 63, w = wid & 3;
  const int xc = blockIdx.x & 7, idx = blockIdx.x >> 3, role = idx >> 4, t = idx & 15, ti = t >> 2, tj = t & 3, ko = (ti + tj) & 3;
  const int st_ = (xc >> 1) * 4 + ti, dt = (xc & 1) * 4 + tj, j = xc * 16 + t;
  STAMP_DECL STAMP(0);
  if (role == 0) {
    if (wid < 8) attn_phase(lds, ws, wid, lane, j STAMP_PASS);
    if (wid < 4) gemm2_tile<0, 0>(lds, x, b2, out, ws, st_, dt, w, lane STAMP_PASS);
    else if (wid < 8) gemm2_tile<1, 0>(lds, x, b2, out, ws, st_, dt, w, lane STAMP_PASS);
    else gemm2_loader<0>(lds0, W, ws, st_, dt, ko, w, lane, j);
  } else {
    if (wid < 4) gemm2_tile<0, 1>(lds, x, b2, out, ws, st_, dt, w, lane STAMP_PASS);
    else if (wid < 8) gemm2_tile<1, 1>(lds, x, b2, out, ws, st_, dt, w, lane STAMP_PASS);
    else gemm2_loader<1>(lds0, W, ws, st_, dt, ko, w, lane, j);
  }
  STAMP(6); STAMP_FLUSH(3);
}

extern "C" void kernel_launch(void* const* d_in, const int* in_sizes, int n_in, void* d_out, int out_size, void* d_ws, size_t ws_size, hipStream_t stream) {
  const u16* x = (const u16*)d_in[0];
  const u16* wqkv = (const u16*)d_in[1];
  const u16* wo = (const u16*)d_in[2];
  const u16* w1 = (const u16*)d_in[4];
  const u16* b1 = (const u16*)d_in[5];
  const u16* w2 = (const u16*)d_in[6];
  const u16* b2 = (const u16*)d_in[7];
  char* ws = (char*)d_ws;
  Weights W; W.wqkv = wqkv; W.w1 = w1; W.w2 = w2; W.wo = wo;
  (void)hipFuncSetAttribute((const void*)k_gemm1, hipFuncAttributeMaxDynamicSharedMemorySize, G1_LDS);
  (void)hipFuncSetAttribute((const void*)k_gemm2, hipFuncAttributeMaxDynamicSharedMemorySize, G2_STAGE * G2_R);
  k_ln<<<SPF_START ? 256 + PF_BLOCKS : 256, 512, 0, stream>>>(x, W, ws);
  k_gemm1<<<256, 768, G1_LDS, stream>>>(W, b1, ws);
  k_gemm2<<<256, 768, G2_STAGE * G2_R, stream>>>(x, W, b2, (u16*)d_out, ws);
}
```

```cpp
#include <hip/hip_runtime.h>
#include <hip/hip_bf16.h>
#include <stdint.h>
#pragma clang diagnostic ignored "-Winline-asm"

typedef unsigned short u16;
typedef short bf16x8 __attribute__((ext_vector_type(8)));
typedef short s16x4 __attribute__((ext_vector_type(4)));
typedef float f32x4 __attribute__((ext_vector_type(4)));
typedef float f32x2 __attribute__((ext_vector_type(2)));
typedef __bf16 b16x2 __attribute__((ext_vector_type(2)));
typedef unsigned u32x4 __attribute__((ext_vector_type(4)));
typedef unsigned u32x2 __attribute__((ext_vector_type(2)));
#define LAS __attribute__((address_space(3)))
#define DI __device__ __forceinline__
typedef __amdgpu_buffer_rsrc_t srd_t;

constexpr int S_ = 2048, D_ = 1024, F_ = 3584;
constexpr int FLS = 16;
constexpr int POLL_SLEEP = 3;
constexpr int LEADN = 7, MPACE = 3, QPACE = 7;
constexpr int PRIO0 = 1, PRIO1 = 2;
constexpr int TS0 = 2, TWQ = 240;
constexpr int TQ1 = 4, TQ2 = 4, WMS = 60;
constexpr int B1J = 4;
constexpr float LMAX = 1.2089258e24f;
constexpr int FL_LN = 1, FL_U = 257, FL_O = 513, FL_KV = 641, FL_P = 769, FL_HI = 897;
constexpr size_t WS_STAT = 196608;
constexpr size_t WS_H = WS_STAT + 65536;
constexpr size_t WS_KV = WS_H + (size_t)S_ * D_ * 2;
constexpr size_t WS_O = WS_KV + (size_t)S_ * D_ * 2;
constexpr size_t WS_U = WS_O + (size_t)S_ * D_ * 2;
constexpr size_t WS_P = WS_U + (size_t)F_ * D_ * 2;
constexpr unsigned OOB = 0x80000000u, SPIN_MAX = 1u << 19;

DI float bf2f(unsigned b) { return __uint_as_float(b << 16); }
DI unsigned pk2(float a, float b) { f32x2 v = {a, b}; return __builtin_bit_cast(unsigned, __builtin_convertvector(v, b16x2)); }
DI float rbf(float a) { return __uint_as_float(pk2(a, 0.f) << 16); }
DI srd_t mk_srd(const void* p, unsigned bytes) { return __builtin_amdgcn_make_buffer_rsrc((void*)p, (short)0, (int)bytes, 0x00020000); }
DI void dma(unsigned lds_dst, unsigned voff, srd_t srd, unsigned soff) {
  asm volatile("s_mov_b32 m0, %0\n\ts_nop 0\n\tbuffer_load_dwordx4 %1, %2, %3 offen lds" ::"s"(lds_dst), "v"(voff), "s"(srd), "s"(soff) : "memory");
}
DI void dma_sc1(unsigned lds_dst, unsigned voff, srd_t srd, unsigned soff) {
  asm volatile("s_mov_b32 m0, %0\n\ts_nop 0\n\tbuffer_load_dwordx4 %1, %2, %3 offen sc1 lds" ::"s"(lds_dst), "v"(voff), "s"(srd), "s"(soff) : "memory");
}
typedef unsigned long long u64;
DI u64 uni64(u64 v) { return ((u64)(unsigned)__builtin_amdgcn_readfirstlane((unsigned)(v >> 32)) << 32) | (unsigned)__builtin_amdgcn_readfirstlane((unsigned)v); }
DI u64* flagp(char* ws, int i) { return (u64*)ws + i * FLS; }
DI void post(char* ws, int i, u64 tag) { __hip_atomic_store(flagp(ws, i), tag, __ATOMIC_RELAXED, __HIP_MEMORY_SCOPE_AGENT); }
DI void signal_wave(LAS unsigned* cnt, char* ws, int i, u64 tag, int lane, unsigned nw = 8u) {
  asm volatile("s_waitcnt vmcnt(0)" ::: "memory");
  if (lane == 0 && __hip_atomic_fetch_add(cnt, 1u, __ATOMIC_RELAXED, __HIP_MEMORY_SCOPE_WORKGROUP) == nw - 1u) post(ws, i, tag);
}
template <int R = 1> DI void wait_words(char* ws, int i0, int n, u64 tag, int lane) {
  u64* f = flagp(ws, i0 + (lane < n ? lane : 0));
#pragma nounroll
  for (unsigned spins = 0; spins < SPIN_MAX; ++spins) {
    bool ok = true;
#pragma unroll
    for (int r = 0; r < R; ++r) ok &= __hip_atomic_load(f + 64 * FLS * r, __ATOMIC_RELAXED, __HIP_MEMORY_SCOPE_AGENT) == tag;
    if (__all(ok)) break;
    __builtin_amdgcn_s_sleep(POLL_SLEEP);
  }
  asm volatile("" ::: "memory");
}
DI void wait_lds(LAS unsigned* w, unsigned want) {
#pragma nounroll
  for (unsigned spins = 0; *(volatile LAS unsigned*)w < want && spins < (SPIN_MAX << 4); ++spins) __builtin_amdgcn_s_sleep(1);
  asm volatile("" ::: "memory");
}
DI void touch(unsigned lds_dst, unsigned voff, srd_t srd, unsigned soff) {
  asm volatile("s_nop 4\n\ts_mov_b32 m0, %0\n\ts_nop 0\n\tbuffer_load_dword %1, %2, %3 offen lds" ::"s"(lds_dst), "v"(voff), "s"(srd), "s"(soff) : "memory");
}
template <int N> DI void stouch(srd_t srd, unsigned off) {
#pragma unroll
  for (int j = 0; j < N; ++j) asm volatile("s_buffer_load_dword s101, %0, %1" ::"s"(srd), "s"(off + j * 64u) : "s101", "memory");
}
DI void st16(srd_t srd, unsigned voff, unsigned soff, u32x4 v) { __builtin_amdgcn_raw_buffer_store_b128(v, srd, voff + soff, 0, 16); }
DI bf16x8 lds_frag(LAS const char* p) { return *(LAS const bf16x8*)p; }
DI bf16x8 lds_tr(LAS const char* p, int step) {
  s16x4 lo = __builtin_amdgcn_ds_read_tr16_b64_v4i16((LAS s16x4*)p);
  s16x4 hi = __builtin_amdgcn_ds_read_tr16_b64_v4i16((LAS s16x4*)(p + step));
  return __builtin_shufflevector(lo, hi, 0, 1, 2, 3, 4, 5, 6, 7);
}
#define MFMA(a, b, c) __builtin_amdgcn_mfma_f32_16x16x32_bf16((a), (b), (c), 0, 0, 0)
#define SBAR() __builtin_amdgcn_sched_barrier(0)
#define BAR() asm volatile("s_barrier" ::: "memory")
#define LGKM0_BAR() asm volatile("s_waitcnt lgkmcnt(0)\n\ts_barrier" ::: "memory")
#define VM_BAR(n) asm volatile("s_waitcnt vmcnt(%0)\n\ts_barrier" ::"n"(n) : "memory")
#define WAIT_BAR(n) asm volatile("s_waitcnt vmcnt(%0) lgkmcnt(0)\n\ts_barrier" ::"n"(n) : "memory")
DI u32x4 pack8(const f32x4& a, const f32x4& b) { return (u32x4){pk2(a[0], a[1]), pk2(a[2], a[3]), pk2(b[0], b[1]), pk2(b[2], b[3])}; }
template <int CTRL> DI float dpp_get(float v) { return __builtin_bit_cast(float, __builtin_amdgcn_update_dpp(0, __builtin_bit_cast(int, v), CTRL, 0xF, 0xF, true)); }
DI void xsum2(float& v, float& w) {
  v += dpp_get<0xB1>(v); w += dpp_get<0xB1>(w); v += dpp_get<0x4E>(v); w += dpp_get<0x4E>(w);
  v += dpp_get<0x141>(v); w += dpp_get<0x141>(w); v += dpp_get<0x140>(v); w += dpp_get<0x140>(w);
  auto a = __builtin_amdgcn_permlane16_swap(__float_as_uint(v), __float_as_uint(v), false, false); v = __uint_as_float(a[0]) + __uint_as_float(a[1]);
  auto c = __builtin_amdgcn_permlane16_swap(__float_as_uint(w), __float_as_uint(w), false, false); w = __uint_as_float(c[0]) + __uint_as_float(c[1]);
  auto b = __builtin_amdgcn_permlane32_swap(__float_as_uint(v), __float_as_uint(v), false, false); v = __uint_as_float(b[0]) + __uint_as_float(b[1]);
  auto d = __builtin_amdgcn_permlane32_swap(__float_as_uint(w), __float_as_uint(w), false, false); w = __uint_as_float(d[0]) + __uint_as_float(d[1]);
}
DI f32x2 unpk(unsigned u) { return (f32x2){__uint_as_float(u << 16), __uint_as_float(u & 0xffff0000u)}; }

constexpr int LN_PITCH = 2048 + 16;
struct Weights { const u16 *wqkv, *w1, *w2, *wo; };
DI f32x2 ln_row(const unsigned (&u)[8], unsigned (&h)[8]) {
  const float piv = __uint_as_float(__builtin_amdgcn_readfirstlane(u[0]) << 16);
  f32x2 d[8], s = {0.f, 0.f}, q = {0.f, 0.f};
#pragma unroll
  for (int j = 0; j < 8; ++j) { d[j] = unpk(u[j]) - piv; s += d[j]; q += d[j] * d[j]; }
  float s1 = s.x + s.y, s2 = q.x + q.y;
  xsum2(s1, s2);
  const float md = s1 * (1.0f / 1024.0f), var = __builtin_fmaxf(s2 * (1.0f / 1024.0f) - md * md, 0.f);
  const float c1 = rbf(piv + md) - piv, a1 = __builtin_amdgcn_rcpf(rbf(__builtin_amdgcn_sqrtf(rbf(rbf(var) + 1e-5f))));
  const float m2 = rbf((md - c1) * a1), a2 = __builtin_amdgcn_rcpf(rbf(__builtin_amdgcn_sqrtf(rbf(rbf(var * a1 * a1) + 1e-5f))));
  const float al = a1 * a2, be = -(c1 * a1 + m2) * a2;
#pragma unroll
  for (int j = 0; j < 8; ++j) { const f32x2 g = d[j] * al + be; h[j] = pk2(g.x, g.y); }
  return (f32x2){c1 + piv, a1};
}
DI f32x2 ln_compute(LAS char* sc, LAS unsigned* cnt, u32x4 v0, u32x4 v1, int w, int lane) {
  unsigned u[8] = {v0[0], v0[1], v0[2], v0[3], v1[0], v1[1], v1[2], v1[3]}, o[8];
  const f32x2 st = ln_row(u, o);
  LAS char* r0 = sc + w * LN_PITCH + lane * 16;
  *(LAS u32x4*)r0 = (u32x4){o[0], o[1], o[2], o[3]};
  *(LAS u32x4*)(r0 + 1024) = (u32x4){o[4], o[5], o[6], o[7]};
  asm volatile("s_waitcnt lgkmcnt(0)" ::: "memory");
  if (lane == 0) __hip_atomic_fetch_add(cnt, 1u, __ATOMIC_RELAXED, __HIP_MEMORY_SCOPE_WORKGROUP);
  return st;
}
DI void ln_store(LAS char* sc, LAS unsigned* cnt, f32x2 st, char* ws, int b, int w, int lane) {
  wait_lds(cnt, 8u);
#pragma unroll
  for (int it = 0; it < 2; ++it) {
    const int df = (w * 2 + it) * 4 + (lane >> 4), q = (lane & 15) >> 2, p = lane & 3;
    LAS const char* r = sc + q * LN_PITCH + (32 * (df >> 1) + 8 * p + 4 * (df & 1)) * 2;
    const bf16x8 v = lds_tr(r, 4 * LN_PITCH);
    st16(mk_srd(ws + WS_H, S_ * D_ * 2), df * 65536 + (lane & 15) * 16, (unsigned)(b >> 2) * 1024u + (b & 3) * 256u, __builtin_bit_cast(u32x4, v));
  }
  if (lane == 0) __hip_atomic_store((u64*)(ws + WS_STAT) + 8 * b + w, __builtin_bit_cast(u64, st), __ATOMIC_RELAXED, __HIP_MEMORY_SCOPE_AGENT);
}
DI void ln_phase(LAS char* sc, LAS unsigned* ctr, const u32x4 (&v)[4], char* ws, u64 tag, int j, int w, int lane) {
  const f32x2 s0 = ln_compute(sc, ctr, v[0], v[1], w, lane);
  ln_store(sc, ctr, s0, ws, j, w, lane);
  if (lane == 0) __hip_atomic_fetch_add(ctr + 3, 1u, __ATOMIC_RELAXED, __HIP_MEMORY_SCOPE_WORKGROUP);
  const f32x2 s1 = ln_compute(sc + 8 * LN_PITCH, ctr + 12, v[2], v[3], w, lane);
  signal_wave(ctr + 1, ws, FL_LN + j, tag, lane);
  ln_store(sc + 8 * LN_PITCH, ctr + 12, s1, ws, 128 + j, w, lane);
  signal_wave(ctr + 13, ws, FL_LN + 128 + j, tag, lane);
}

DI f32x2 gelu2(f32x2 v) {
  const f32x2 t = v * (v * v * (-0.044715f * 2.302208198f) + (-2.302208198f));
  f32x2 e; e.x = __builtin_amdgcn_exp2f(t.x); e.y = __builtin_amdgcn_exp2f(t.y);
  e = e + 1.0f;
  f32x2 r; r.x = __builtin_amdgcn_rcpf(e.x); r.y = __builtin_amdgcn_rcpf(e.y);
  return v * r;
}
DI u32x2 gelu4(f32x4 v) {
  const f32x2 lo = gelu2((f32x2){v[0], v[1]}), hi = gelu2((f32x2){v[2], v[3]});
  return (u32x2){pk2(lo.x, lo.y), pk2(hi.x, hi.y)};
}

template <int PER, int J, int N> DI int kord(int t, int o) { static_assert(J == 0 || (N % PER == 0 && J % PER == 0), "bijection"); const int k = t + ((t & (PER - 1)) == o ? J : 0); return k >= N ? k - N : k; }
constexpr int G2_STAGE = 32768, G2_R = 5, G2_NA = 34, G2_N1 = 56 - G2_NA, G2_NB = G2_N1 + 16;
constexpr int B1_STAGE = 45056, B1_R = 3, B1_NS = 32, B1_XCH = B1_STAGE * B1_R;
constexpr int QK_NS = 32, QK_W = 131072, QCTR = 163840 - 256;
constexpr int K2_LDS = 163840;
constexpr int LN_SC = 98304, CTR = 147456 - 256;

struct Warm {
  static constexpr int CQ = 4 + TWQ / 12, C1 = 4 + (TS0 * 256 - TWQ) / 28;
  static constexpr int W2T = (2048 - (TQ1 + TQ2) * 128 + 19) / 20, W2M = W2T * WMS / 100;
  unsigned off, step; srd_t srd; int lw, n, te;
  static DI void first(const Weights& W, unsigned scratch, int j, int w, int lane) {
#pragma unroll
    for (int v = w; v < 2 * TS0; v += 4) { const int pc = v * 128 + j; touch(scratch, lane * 128, pc < TWQ ? mk_srd(W.wqkv, D_ * 3 * D_ * 2) : mk_srd(W.w1, S_ * F_ * 2), (unsigned)(pc < TWQ ? 48 + pc : 112 + pc - TWQ) * 8192u); }
  }
  DI void init(const Weights& W, int blk, int w) {
    lw = blk * 4 + w; te = 64 + (blk < 128 ? W2M : W2T - W2M);
    const int l2 = (lw >> 9) | ((lw & 511) << 1); const bool a = l2 < 717;
    srd = a ? mk_srd(W.w1, S_ * F_ * 2) : mk_srd(W.wqkv, D_ * 3 * D_ * 2);
    n = a ? C1 : CQ; step = a ? 28u * 8192u : 12u * 8192u; off = (a ? l2 : l2 - 717) * 320u + n * step;
  }
  DI void tick(const Weights& W) {
    if (n >= te) return;
    unsigned pend; asm volatile("s_getreg_b32 %0, hwreg(HW_REG_IB_STS, 8, 4)" : "=s"(pend));
    if (pend > 10u) return;
    if (n == 64) { srd = mk_srd(W.w2, F_ * S_ * 2); step = 163840u; off = (lw & 511) * 320u + (lw >> 9) * (W2M * 163840u); }
    stouch<5>(srd, off); off += step; ++n;
  }
  DI void drain() { asm volatile("s_waitcnt lgkmcnt(0)" ::: "memory"); }
  DI void paced(const Weights& W, int n_) {
#pragma nounroll
    for (int r = 0; r < n_; ++r) { tick(W); __builtin_amdgcn_s_sleep(4); }
  }
};

template <int ROLE> struct G2Load {
  static constexpr int N1 = ROLE ? G2_N1 : G2_NA, NS = ROLE ? G2_NB : G2_NA, SB = ROLE ? 2 : 4;
  static constexpr int slot_of(int s) { return ((SB - s) % G2_R + G2_R) % G2_R; }
  static constexpr int XS = slot_of(NS - 3), PS = slot_of(NS - 4);
  static_assert(G2_N1 <= 24, "the short role starts on at least eight T0 tiles");
  static DI int t0(int k) { return 7 * (k >> 2) + (k & 1) + 4 * ((k >> 1) & 1); }
  static DI int ktile(int s) { if (ROLE) return t0(s); if (s < 32 - G2_N1) return t0(G2_N1 + s); s -= 32 - G2_N1; const int a = (s * 43) >> 7, r = s - 3 * a; return 7 * a + (r == 2 ? 6 : r + 2); }
  unsigned lds0, vofa, vow2, vowo; srd_t sru, srw2, sro, srwo; int st_, dt, w;
  DI void init(unsigned lds0_, const Weights& W, char* ws, int st, int dt_, int w_, int lane) {
    lds0 = lds0_; st_ = st; dt = dt_; w = w_;
    sru = mk_srd(ws + WS_U, F_ * D_ * 2); srw2 = mk_srd(W.w2, F_ * S_ * 2); sro = mk_srd(ws + WS_O, S_ * D_ * 2); srwo = mk_srd(W.wo, D_ * D_ * 2);
    const unsigned swz = ((lane & 7) ^ (((lane >> 4) & 3) << 1)) * 16;
    vofa = lane * 16; vow2 = (lane >> 3) * 4096 + swz; vowo = (lane >> 3) * 2048 + swz;
  }
  DI void w1(int s, int slot) {
    const unsigned dst = lds0 + slot * G2_STAGE + 16384 + w * 2048, sw = (unsigned)(64 * ktile(s) + 16 * w) * 4096u + st_ * 256u;
#pragma unroll
    for (int i = 0; i < 4; ++i) dma(dst + (i >> 1) * 8192 + (i & 1) * 1024, vow2, srw2, sw + (i & 1) * (8u * 4096u) + (i >> 1) * 128u);
  }
  DI void a1(int s, int slot) {
    const unsigned dst = lds0 + slot * G2_STAGE + w * 4096, sa = ((unsigned)(dt * 8 + 2 * w) * 112u + 2u * ktile(s)) * 1024u;
#pragma unroll
    for (int i = 0; i < 4; ++i) dma_sc1(dst + i * 1024, vofa, sru, sa + (i >> 1) * (112u * 1024u) + (i & 1) * 1024u);
  }
  DI void wpart(int s, int slot) {
    if (!ROLE || s < N1) w1(s, slot);
    else {
      const unsigned dst = lds0 + slot * G2_STAGE + 16384 + w * 2048, sw = (unsigned)(64 * (s - N1) + 16 * w) * 2048u + dt * 256u;
#pragma unroll
      for (int i = 0; i < 4; ++i) dma(dst + (i >> 1) * 8192 + (i & 1) * 1024, vowo, srwo, sw + (i & 1) * (8u * 2048u) + (i >> 1) * 128u);
    }
  }
  DI void apart(int s, int slot) {
    if (!ROLE || s < N1) a1(s, slot);
    else {
      const unsigned dst = lds0 + slot * G2_STAGE + w * 4096, sa = ((unsigned)(st_ * 8 + 2 * w) * 32u + 2u * (s - N1)) * 1024u;
#pragma unroll
      for (int i = 0; i < 4; ++i) dma_sc1(dst + i * 1024, vofa, sro, sa + (i >> 1) * (32u * 1024u) + (i & 1) * 1024u);
    }
  }
  DI void issue(int s, int slot) { wpart(s, slot); apart(s, slot); }
  DI void wait_u(char* ws, u64 tag, int lane) { wait_words(ws, FL_U + dt * 32 + (ROLE ? 0 : G2_N1 / 2), ROLE ? G2_N1 / 2 : 16 - G2_N1 / 2, tag, lane); }
  DI void run(const u16* x, const Weights& W, Warm& wm, char* ws, u64 tag, int lane) {
    int slot = (SB + 2) % G2_R;
    for (int s = 3; s < NS - 1; s += 2) {
      issue(s, slot);
      slot = slot == 0 ? G2_R - 1 : slot - 1;
      wm.tick(W);
      issue(s + 1, slot);
      slot = slot == 0 ? G2_R - 1 : slot - 1;
      wm.tick(W);
      VM_BAR(8);
    }
    issue(NS - 1, slot);
    wm.drain();
    VM_BAR(0);
    if (ROLE) {
      const int tile = st_ * 8 + dt;
      const srd_t srx = mk_srd(x, S_ * D_ * 2), srp = mk_srd(ws + WS_P + (size_t)tile * 32768, 32768);
      const u64 seen = __hip_atomic_load(flagp(ws, FL_P + tile), __ATOMIC_RELAXED, __HIP_MEMORY_SCOPE_AGENT);
      auto partial = [&]() {
#pragma unroll
        for (int i = 0; i < 8; ++i) dma_sc1(lds0 + PS * G2_STAGE + w * 8192 + i * 1024, vofa, srp, w * 8192u + i * 1024u);
      };
      const bool early = __all(seen == tag);
      asm volatile("" ::: "memory");
      if (early) partial();
      const unsigned vx = (lane >> 3) * 2048 + (((lane & 7) ^ (lane >> 3)) * 16);
#pragma unroll
      for (int i = 0; i < 8; ++i)
        dma(lds0 + XS * G2_STAGE + ((i >> 2) * 4 + w) * 4096 + (i & 3) * 1024, vx, srx, (unsigned)(st_ * 128 + (w & 1) * 64 + i * 8) * 2048u + dt * 256u + (w >> 1) * 128u);
      BAR();
      if (early) { VM_BAR(0); return; }
      const bool now = __all(__hip_atomic_load(flagp(ws, FL_P + tile), __ATOMIC_RELAXED, __HIP_MEMORY_SCOPE_AGENT) == tag);
      asm volatile("" ::: "memory");
      if (now) { partial(); VM_BAR(8); } else VM_BAR(0);
      if (!now) { wait_words(ws, FL_P + tile, 1, tag, lane); partial(); }
      VM_BAR(0);
    }
  }
};

DI void m_loader(LAS char* lds, unsigned lds0, const u16* x, const Weights& W, char* ws, int dt, int ft, int st2, int w, int lane, int j) {
  const bool odd = ft & 1;
  const int ko = (dt + ft) & 3;
  const srd_t sra = mk_srd(ws + WS_H, S_ * D_ * 2), srw = mk_srd(W.w1, S_ * F_ * 2);
  const unsigned va = lane * 16, vw = (8 * (lane >> 5) + ((lane >> 3) & 3)) * (F_ * 2) + (((lane & 7) ^ (((lane >> 4) & 3) << 1)) * 16);
  const unsigned vh = (8 * (lane >> 4) + ((lane >> 2) & 3)) * (F_ * 2) + (((lane & 3) ^ (((lane >> 4) & 1) << 1)) * 16);
  const unsigned sba = (unsigned)(dt * 8 + 2 * w) * 65536u, sbw = (unsigned)(32 * (w >> 1) + 4 * (w & 1)) * (F_ * 2) + 448u * ft + (odd ? 64u : 0u);
  const unsigned sbh = (unsigned)(32 * (w >> 1) + 4 * (w & 1)) * (F_ * 2) + 448u * ft + (odd ? 0u : 384u);
  auto ldA = [&](int t, int slot) {
    const unsigned so = sba + kord<4, B1J, B1_NS>(t, ko) * 2048u, dst = lds0 + slot * B1_STAGE + w * 4096;
#pragma unroll
    for (int i = 0; i < 4; ++i) dma_sc1(dst + i * 1024, va, sra, so + (i >> 1) * 65536u + (i & 1) * 1024u);
  };
  auto ldW0 = [&](int t, int slot, int i0 = 0, int i1 = 4) {
    const unsigned so = sbw + (unsigned)kord<4, B1J, B1_NS>(t, ko) * (64u * F_ * 2), dst = lds0 + slot * B1_STAGE + 16384 + w * 2048;
#pragma unroll
    for (int i = i0; i < i1; ++i) dma(dst + (i >> 1) * 8192 + (i & 1) * 1024, vw, srw, so + (i & 1) * (16u * F_ * 2) + (i >> 1) * 128u);
  };
  auto ldF = [&](int t, int slot) {
    const unsigned kb = (unsigned)kord<4, B1J, B1_NS>(t, ko) * (64u * F_ * 2), dst = lds0 + slot * B1_STAGE;
#pragma unroll
    for (int i = 0; i < 2; ++i) dma(dst + 32768 + w * 2048 + i * 1024, vw, srw, sbw + kb + i * (16u * F_ * 2) + 256u);
  };
  auto ldH = [&](int t, int slot) { dma(lds0 + slot * B1_STAGE + 40960 + w * 1024, vh, srw, sbh + (unsigned)kord<4, B1J, B1_NS>(t, ko) * (64u * F_ * 2)); };
  Warm::first(W, lds0 + CTR - 256, j, w, lane);
  wait_lds((LAS unsigned*)(lds + CTR) + 3, 8u);
  const u64 tag = uni64(*(volatile LAS u64*)(lds + CTR + 32));
  ldW0(0, 0); ldF(0, 0); ldH(0, 0);
  Warm wm; wm.init(W, j, w);
  wm.paced(W, LEADN);
  BAR();
  ldA(0, 0); ldW0(1, 1); ldA(1, 1); ldF(1, 1); ldH(1, 1);
  VM_BAR(11);
  int sl = 2;
  for (int s = 0; s < B1_NS - 2; ++s) {
    wm.tick(W);
    ldW0(s + 2, sl); wm.tick(W); ldA(s + 2, sl); wm.tick(W); ldF(s + 2, sl); ldH(s + 2, sl);
    VM_BAR(11);
    sl = sl == B1_R - 1 ? 0 : sl + 1;
  }
  VM_BAR(0);
  BAR();
  wm.drain();
  G2Load<1> g; g.init(lds0, W, ws, st2, dt, w, lane);
#pragma unroll
  for (int s = 0; s < 3; ++s) g.w1(s, 2 - s);
  wm.paced(W, MPACE); wm.drain();
  if (w == 0) {
    wait_lds((LAS unsigned*)(lds + CTR) + 2, 4u);
    g.wait_u(ws, tag, lane);
    if (lane == 0) *(volatile LAS unsigned*)((LAS unsigned*)(lds + CTR) + 10) = 1u;
  } else wait_lds((LAS unsigned*)(lds + CTR) + 10, 1u);
#pragma unroll
  for (int s = 0; s < 3; ++s) g.a1(s, 2 - s);
  VM_BAR(4);
  g.run(x, W, wm, ws, tag, lane);
}
template <int GRP>
DI void b1_tile(LAS char* lds, const u16* b1, char* ws, u64 tag, int dt, int ft, int w, int lane, bool first) {
  constexpr int NW = GRP == 0 ? 4 : 3;
  const int wr = w >> 1, wc = w & 1, g = lane >> 4, q = (lane & 15) >> 2, p = lane & 3;
  const bool odd = ft & 1;
  const int r = 4 * (g & 1) + q;
  int woff[NW];
#pragma unroll
  for (int i = 0; i < NW; ++i) {
    if (GRP == 0) woff[i] = 16384 + (wc * 8 + (g >> 1)) * 1024 + r * 128 + ((i ^ ((r >> 1) & 3)) * 32) + p * 8;
    else if (i < 2) woff[i] = 16384 + (16 + (g >> 1)) * 1024 + r * 128 + (((2 * wc + i) ^ ((r >> 1) & 3)) * 32) + p * 8;
    else woff[i] = 40960 + (4 * g + q) * 64 + ((wc ^ (g & 1)) * 32) + p * 8;
  }
  const int aoff = wr * 8192 + lane * 16;
  int fgs[2];
#pragma unroll
  for (int pr = 0; pr < 2; ++pr) fgs[pr] = (GRP == 1 && pr == 1) ? 7 * ft + (odd ? 0 : 6) : 7 * ft + (GRP == 0 ? 2 * wc + pr : 4 + wc) + (odd ? 1 : 0);
  u32x2 bias[NW];
#pragma unroll
  for (int i = 0; i < NW; ++i) bias[i] = *(const u32x2*)(b1 + fgs[i >> 1] * 32 + (i == 2 && GRP == 1 ? wc : i & 1) * 16 + 4 * g);
  LAS unsigned* xf = (LAS unsigned*)(lds + CTR) + 4;
  BAR(); BAR();
  SBAR();
  f32x4 acc[NW][4];
#pragma unroll
  for (int i = 0; i < NW; ++i) {
    asm volatile("" : "+v"(bias[i]));
    const f32x2 b0 = unpk(bias[i][0]), b1v = unpk(bias[i][1]);
#pragma unroll
    for (int j = 0; j < 4; ++j) acc[i][j] = (f32x4){b0.x, b0.y, b1v.x, b1v.y};
  }
  int rs = 0, s = 0;
  auto step = [&]() {
    LAS const char* st = lds + rs * B1_STAGE;
    bf16x8 A[2][4], W[2][NW];
#pragma unroll
    for (int ks = 0; ks < 2; ++ks)
#pragma unroll
      for (int i = 0; i < 4; ++i) {
        const bool late = GRP == 0 && ks == 1;
        if (i == 0 || (!late && i > 1 && i - 1 < NW - 1)) { const int wi = i == 0 ? 0 : i - 1; W[ks][wi] = lds_tr(st + woff[wi] + ks * 4096, 2048); }
        A[ks][i] = lds_frag(st + aoff + i * 2048 + ks * 1024);
        if (i == 3 && !late) W[ks][NW - 1] = GRP == 1 ? lds_tr(st + woff[NW - 1] + ks * 2048, 1024) : lds_tr(st + woff[NW - 1] + ks * 4096, 2048);
        asm volatile("" ::: "memory");
      }
    if (GRP == 1) LGKM0_BAR();
    SBAR();
    __builtin_amdgcn_s_setprio(GRP ? PRIO1 : PRIO0);
#pragma unroll
    for (int ks = 0; ks < 2; ++ks)
#pragma unroll
      for (int i = 0; i < NW; ++i) {
#pragma unroll
        for (int j = 0; j < 4; ++j) acc[i][j] = MFMA(W[ks][i], A[ks][j], acc[i][j]);
        if (GRP == 0 && ks == 0 && i < 3) { W[1][i + 1] = lds_tr(st + woff[i + 1] + 4096, 2048); SBAR(); }
      }
    __builtin_amdgcn_s_setprio(0);
    SBAR();
    if (GRP == 0) LGKM0_BAR();
    rs = rs == B1_R - 1 ? 0 : rs + 1; ++s;
  };
  if (GRP == 0) {
    u64 seen[3] = {0, 0, 0};
    if (w == 0) {
#pragma unroll
      for (int k = 0; k < 3; ++k) seen[k] = __hip_atomic_load(flagp(ws, FL_LN + 64 * (k + 1) + lane), __ATOMIC_RELAXED, __HIP_MEMORY_SCOPE_AGENT);
    }
    _Pragma("nounroll") while (s < (B1J ? 1 : 3)) step();
    asm volatile("" : "+v"(seen[0]), "+v"(seen[1]), "+v"(seen[2]));
    if (w == 0) {
      if (!__all(seen[0] == tag && seen[1] == tag && seen[2] == tag)) wait_words<3>(ws, FL_LN + 64, 64, tag, lane);
      if (first) { wait_words<2>(ws, FL_HI, 64, tag, lane); if (lane == 0) post(ws, 0, tag); }
    }
  }
  _Pragma("nounroll") while (s < B1_NS) step();
  const srd_t sru = mk_srd(ws + WS_U, F_ * D_ * 2);
  const int df0 = dt * 8 + wr * 4;
  u32x2 keep[2];
  if (GRP == 1) {
    LAS u32x2* xo = (LAS u32x2*)(lds + B1_XCH + w * 1024) + lane;
#pragma unroll
    for (int k = 0; k < 2; ++k) {
      const u32x2 lo = gelu4(acc[2][k]), hi = gelu4(acc[2][2 + k]);
      xo[k * 64] = wc ? lo : hi; keep[k] = wc ? hi : lo;
    }
    asm volatile("s_waitcnt lgkmcnt(0)" ::: "memory");
    if (lane == 0) *(volatile LAS unsigned*)(xf + w) = 1u;
  }
#pragma unroll
  for (int pr = 0; pr < (GRP == 0 ? 2 : 1); ++pr)
#pragma unroll
    for (int j = 0; j < 4; ++j) {
      const u32x2 lo = gelu4(acc[2 * pr][j]), hi = gelu4(acc[2 * pr + 1][j]);
      st16(sru, lane * 16, (unsigned)((df0 + j) * 112 + fgs[pr]) * 1024u, (u32x4){lo[0], lo[1], hi[0], hi[1]});
    }
  if (GRP == 1) {
    wait_lds(xf + (w ^ 1), 1u);
    LAS const u32x2* xi = (LAS const u32x2*)(lds + B1_XCH + (w ^ 1) * 1024) + lane;
#pragma unroll
    for (int k = 0; k < 2; ++k) {
      const u32x2 o = xi[k * 64], lo = wc ? o : keep[k], hi = wc ? keep[k] : o;
      st16(sru, lane * 16, (unsigned)((df0 + 2 * wc + k) * 112 + fgs[1]) * 1024u, (u32x4){lo[0], lo[1], hi[0], hi[1]});
    }
  }
  signal_wave((LAS unsigned*)(lds + CTR) + (GRP ? 11 : 2), ws, FL_U + dt * 32 + GRP * 16 + ft, tag, lane, 4u);
}

DI float mx3(float a, float b, float c) { return __builtin_fmaxf(__builtin_fmaxf(a, b), c); }
DI float max_rows(float m) {
  auto a = __builtin_amdgcn_permlane16_swap(__float_as_uint(m), __float_as_uint(m), false, false); m = __builtin_fmaxf(__uint_as_float(a[0]), __uint_as_float(a[1]));
  auto b = __builtin_amdgcn_permlane32_swap(__float_as_uint(m), __float_as_uint(m), false, false); return __builtin_fmaxf(__uint_as_float(b[0]), __uint_as_float(b[1]));
}
DI f32x4 splat(float v) { return (f32x4){v, v, v, v}; }
template <bool FIRST>
DI void attn_unit(LAS const char* kv, const bf16x8 (&Q)[2], srd_t sro, unsigned ooff, int g0, int lane) {
  const float NEG = -1e30f;
  const f32x4 z = splat(0.f);
  constexpr int G0 = FIRST ? 1 : 0;
  auto live = [](int kg, int t) { return !((kg == 0 && t == 1) || (kg == 8 && t == 2)); };
  auto qk = [&](f32x4 (&sc)[4], const bf16x8 (&K)[2], int kg, const f32x4 (&c)[2], const f32x4 (&clo)[2], const f32x4 (&chi)[2]) {
#pragma unroll
    for (int t = 0; t < 4; ++t) {
      const int h = t >> 1, b = t & 1;
      if (!live(kg, t)) continue;
      const f32x4 cc = (FIRST && kg < 8) ? splat(kg >= g0 ? c[b][0] : NEG) : c[b];
      sc[t] = MFMA(K[h], Q[b], (kg == 0 && h == b) ? clo[b] : (kg == 8 && h == b) ? chi[b] : cc);
    }
  };
  auto ldk = [&](bf16x8 (&K)[2], int kg) { K[0] = lds_frag(kv + kg * 4096); K[1] = lds_frag(kv + kg * 4096 + 1024); };
  const bf16x8 ones = {0x3f80, 0x3f80, 0x3f80, 0x3f80, 0x3f80, 0x3f80, 0x3f80, 0x3f80};
  f32x4 o[2][2], l[2];
#pragma nounroll
  for (int exact = 0; ; exact = 1) {
    asm volatile("" : "+v"(lane), "+s"(g0)::"memory");
    const int i = lane & 15, g4 = (lane >> 4) * 4;
    f32x4 lo, hi;
#pragma unroll
    for (int r = 0; r < 4; ++r) { lo[r] = g4 + r > i ? 0.f : NEG; hi[r] = g4 + r > i ? NEG : 0.f; }
    bf16x8 K[3][2]; f32x4 sc[2][4];
    float m[2] = {NEG, NEG};
    {
      const f32x4 c[2] = {z, z}, clo[2] = {lo, lo}, chi[2] = {hi, hi};
      if (exact) {
#pragma unroll
        for (int kg = G0; kg < 8; ++kg) {
          ldk(K[0], kg); qk(sc[0], K[0], kg, c, clo, chi);
#pragma unroll
          for (int t = 0; t < 4; ++t) if (live(kg, t)) { const f32x4 v = sc[0][t]; m[t & 1] = mx3(mx3(m[t & 1], v[0], v[1]), v[2], v[3]); }
          asm volatile("" : "+v"(m[0]), "+v"(m[1])::"memory");
        }
      }
      bf16x8 Kd[2]; ldk(Kd, 8);
      ldk(K[0], G0); ldk(K[1], G0 + 1); ldk(K[2], G0 + 2);
      qk(sc[0], Kd, 8, c, clo, chi);
#pragma unroll
      for (int t = 0; t < 4; ++t) if (live(8, t)) { const f32x4 v = sc[0][t]; m[t & 1] = mx3(mx3(m[t & 1], v[0], v[1]), v[2], v[3]); }
      asm volatile("" : "+v"(m[0]), "+v"(m[1])::"memory");
    }
    f32x4 c[2], clo[2], chi[2];
#pragma unroll
    for (int b = 0; b < 2; ++b) { c[b] = splat(-max_rows(m[b])); clo[b] = c[b] + lo; chi[b] = c[b] + hi; o[b][0] = z; o[b][1] = z; l[b] = z; }
    u32x4 P[2][2]; bf16x8 V[2][2];
    SBAR();
#pragma unroll
    for (int kg = G0; kg < 11; ++kg) {
      if (kg < 9) qk(sc[kg & 1], K[(kg - G0) % 3], kg, c, clo, chi);
      if (kg + 3 < 9) ldk(K[(kg - G0) % 3], kg + 3);
      if (kg - 1 >= G0 && kg - 1 < 9) {
        V[(kg - 1) & 1][0] = lds_frag(kv + (kg - 1) * 4096 + 2048); V[(kg - 1) & 1][1] = lds_frag(kv + (kg - 1) * 4096 + 3072);
#pragma unroll
        for (int t = 0; t < 4; ++t) {
          const f32x4 v = sc[(kg - 1) & 1][t]; const bool lv = live(kg - 1, t);
          P[(kg - 1) & 1][t & 1][(t >> 1) * 2] = lv ? pk2(__builtin_amdgcn_exp2f(v[0]), __builtin_amdgcn_exp2f(v[1])) : 0u;
          P[(kg - 1) & 1][t & 1][(t >> 1) * 2 + 1] = lv ? pk2(__builtin_amdgcn_exp2f(v[2]), __builtin_amdgcn_exp2f(v[3])) : 0u;
        }
      }
      if (kg - 2 >= G0) {
#pragma unroll
        for (int b = 0; b < 2; ++b) {
          const bf16x8 pb = __builtin_bit_cast(bf16x8, P[kg & 1][b]);
          o[b][0] = MFMA(V[kg & 1][0], pb, o[b][0]); o[b][1] = MFMA(V[kg & 1][1], pb, o[b][1]); l[b] = MFMA(ones, pb, l[b]);
        }
      }
#define SG(m, n) __builtin_amdgcn_sched_group_barrier(m, n, 0)
#pragma unroll
      for (int t = 0; t < 8; ++t) { SG(0x400, 2); SG(0x8, 1); SG(0x2, 1); }
      if (kg - 1 >= G0 && kg - 1 < 9) asm volatile("" : "+v"(P[(kg - 1) & 1][0]), "+v"(P[(kg - 1) & 1][1])::"memory");
      SBAR();
    }
    if (exact || __all(l[0][0] <= LMAX && l[1][0] <= LMAX)) break;
  }
#pragma unroll
  for (int b = 0; b < 2; ++b) {
    const float inv = __builtin_amdgcn_rcpf(l[b][0]);
    __builtin_amdgcn_raw_buffer_store_b128(pack8(o[b][0] * inv, o[b][1] * inv), sro, lane * 16 + ooff + b * 32768u, 0, 16);
  }
}

DI void q_loader(LAS char* lds, unsigned lds0, const u16* x, const Weights& W, char* ws, int sc, int head, int st2, int dt2, int w, int lane, int j) {
  const srd_t sra = mk_srd(x, S_ * D_ * 2), srw = mk_srd(W.wqkv, D_ * 3 * D_ * 2);
  const unsigned va = lane * 16, vx = (lane >> 3) * 2048 + (((lane & 7) ^ (lane >> 3)) * 16);
  const unsigned vw = (8 * (lane >> 4) + ((lane >> 2) & 3)) * 6144 + (((lane & 3) ^ (((lane >> 4) & 1) << 1)) * 16);
  const unsigned sba = (unsigned)(sc * 512 + w * 32) * 2048u, sbw = w * 2048u + head * 64u;
  auto ldX = [&](int grp, int pr, int i0, int i1) {
    const unsigned so = sba + grp * (128u * 2048u) + (pr & 15) * 128u, dst = lds0 + (grp * 2 + (pr & 1)) * 32768 + w * 8192;
#pragma unroll
    for (int i = i0; i < i1; ++i) dma(dst + i * 1024, vx, sra, so + ((i >> 2) * 256 + (i & 3) * 8) * 2048u);
  };
  auto ldW = [&](int t) {
    const unsigned so = sbw + (t & 31) * (32u * 6144u), dst = lds0 + QK_W + (t & 3) * 6144 + w * 2048;
    if (w < 3) { dma(dst, vw, srw, so); dma(dst + 1024, vw, srw, so + 4u * 6144u); }
  };
  ldW(0); ldW(1); ldX(0, 0, 0, 8); ldX(1, 0, 0, 8); ldW(2); ldX(1, 1, 0, 8);
  Warm wm; wm.init(W, 128 + j, w);
  VM_BAR(8);
  const u64 tag = uni64(*(volatile LAS u64*)(lds + QCTR + 32));
  for (int p = 0; p < QK_NS - 2; ++p) {
    const int grp = p & 1, pr = ((p + 1) >> 1) + 1;
    ldW(p + 3); ldX(grp, pr, 0, 2); wm.tick(W); ldX(grp, pr, 2, 6); wm.tick(W); ldX(grp, pr, 6, 8);
    VM_BAR(8);
  }
  static_assert(TQ1 == 4 && TQ2 == 4, "one touch per loader wave and round");
  auto warm2 = [&](int i, unsigned scratch) {
    const int back = (TQ1 + TQ2 - i) * 128 - j;
    touch(scratch, lane * 128, back <= 256 ? mk_srd(W.wo, D_ * D_ * 2) : mk_srd(W.w2, F_ * S_ * 2), (unsigned)(back <= 256 ? 256 - back : 1792 + 256 - back) * 8192u);
  };
  warm2(w, lds0 + QCTR - 256);
  VM_BAR(1);
  BAR();
  BAR();
  VM_BAR(0);
  wm.drain();
  G2Load<0> g; g.init(lds0, W, ws, st2, dt2, w, lane);
  {
    if (sc > 0) wait_words(ws, FL_KV + head * 4 + sc - 1, 1, tag, lane);
    const srd_t skv = mk_srd(ws + WS_KV, S_ * D_ * 2);
    const unsigned so = sc > 0 ? (unsigned)(head * 4 + sc - 1) * 32768u + w * 8192u : OOB;
#pragma unroll
    for (int i = 0; i < 8; ++i) dma_sc1(lds0 + w * 8192 + i * 1024, va, skv, so + i * 1024u);
    g.w1(0, 4); g.w1(1, 3);
    asm volatile("s_waitcnt vmcnt(8)" ::: "memory");
    warm2(TQ1 + w, lds0 + 3 * G2_STAGE + w * 4096 + 256);
  }
  BAR();
  g.w1(2, 2);
  wm.paced(W, QPACE);
  g.wait_u(ws, tag, lane);
  g.a1(0, 4); g.a1(1, 3); g.a1(2, 2);
  VM_BAR(4);
  g.run(x, W, wm, ws, tag, lane);
}
DI void q_tile(LAS char* lds, char* ws, u64 tag, int sc, int head, int wid, int lane) {
  const int g = lane >> 4, q = (lane & 15) >> 2, p = lane & 3;
  const bool grp = wid >= 4;
  int woff[2];
#pragma unroll
  for (int i = 0; i < 2; ++i) woff[i] = QK_W + (4 * g + q) * 64 + ((i ^ (g & 1)) * 32) + p * 8;
  const int aoff = (lane & 15) * 128 + ((g ^ (lane & 7)) * 16), abase = (wid >> 2) * 65536 + (wid & 3) * 8192;
  const int cf = wid < 6 ? wid : wid - 6, coff = (woff[0] ^ ((cf & 1) * 32)) + (cf >> 1) * 2048;
  const bf16x8 ones = {0x3f80, 0x3f80, 0x3f80, 0x3f80, 0x3f80, 0x3f80, 0x3f80, 0x3f80};
  f32x4 acc[6][4], cs = splat(0.f);
#pragma unroll
  for (int i = 0; i < 6; ++i)
#pragma unroll
    for (int j = 0; j < 4; ++j) acc[i][j] = splat(0.f);
  LGKM0_BAR();
  int s = 0;
  auto step = [&]() {
    const int k = s + (grp ? 1 : 0);
    LAS const char* wp = lds + (k & 3) * 6144;
    LAS const char* xp = lds + abase + ((k >> 1) & 1) * 32768 + (aoff ^ ((k & 1) * 64));
    bf16x8 A[4], W[6];
    W[0] = lds_tr(wp + woff[0], 1024);
#pragma unroll
    for (int j = 0; j < 4; ++j) A[j] = lds_frag(xp + j * 2048);
    asm volatile("" ::: "memory");
#pragma unroll
    for (int i = 1; i < 6; ++i) W[i] = lds_tr(wp + woff[i & 1] + (i >> 1) * 2048, 1024);
    const bf16x8 Wc = lds_tr(wp + coff, 1024);
    if (grp) { LGKM0_BAR(); SBAR(); __builtin_amdgcn_s_setprio(PRIO1); } else __builtin_amdgcn_s_setprio(PRIO0);
    SBAR();
#pragma unroll
    for (int i = 0; i < 6; ++i)
#pragma unroll
      for (int j = 0; j < 4; ++j) acc[i][j] = i < 4 ? MFMA(W[i], A[j], acc[i][j]) : MFMA(A[j], W[i], acc[i][j]);
    cs = MFMA(ones, Wc, cs);
    __builtin_amdgcn_s_setprio(0);
    SBAR();
    if (!grp) LGKM0_BAR();
    ++s;
  };
  auto rowf = [&]() { int l = lane; asm volatile("" : "+v"(l)); return sc * 512 + (l >> 5) * 256 + wid * 32 + (l & 31); };
  _Pragma("nounroll") while (s < 12) step();
  u64 sv = __hip_atomic_load(flagp(ws, FL_LN + (rowf() >> 3)), __ATOMIC_RELAXED, __HIP_MEMORY_SCOPE_AGENT);
  _Pragma("nounroll") while (s < 16) step();
#pragma nounroll
  for (unsigned spins = 0; !__all(sv == tag) && spins < SPIN_MAX; ++spins) { __builtin_amdgcn_s_sleep(POLL_SLEEP); sv = __hip_atomic_load(flagp(ws, FL_LN + (rowf() >> 3)), __ATOMIC_RELAXED, __HIP_MEMORY_SCOPE_AGENT); }
  asm volatile("" ::: "memory");
  sv = __hip_atomic_load((u64*)(ws + WS_STAT) + rowf(), __ATOMIC_RELAXED, __HIP_MEMORY_SCOPE_AGENT);
  _Pragma("nounroll") while (s < QK_NS) step();
  LAS float* csl = (LAS float*)(lds + 3 * G2_STAGE + 1024);
  LAS float* stl = (LAS float*)(lds + 3 * G2_STAGE + 8192 + 1024) + wid * 128;
  { const f32x2 t = __builtin_bit_cast(f32x2, sv); stl[lane] = t.y; stl[64 + lane] = -t.x * t.y; }
  if (wid < 6 && lane < 16) csl[wid * 16 + lane] = cs[0];
  LGKM0_BAR();
  const float qs = 0.17677669529663687f * 1.4426950408889634f;
  const srd_t skv = mk_srd(ws + WS_KV + (size_t)(head * 4 + sc) * 32768, sc < 3 ? 32768 : 0);
  f32x4 c4[4];
#pragma unroll
  for (int i = 0; i < 4; ++i) c4[i] = *(LAS const f32x4*)(csl + i * 16 + 4 * g);
  const float cv[2] = {csl[64 + (lane & 15)], csl[80 + (lane & 15)]};
  bf16x8 Q[2], Qn[2];
#pragma unroll
  for (int u = 1; u >= 0; --u) {
    LAS char* kv = lds + (8 + 8 * u + wid) * 4096 + lane * 16;
    float sa[2], sb[2]; f32x4 va[2], vb[2];
#pragma unroll
    for (int h = 0; h < 2; ++h) {
      const int r0 = (2 * u + h) * 16;
      sa[h] = stl[r0 + (lane & 15)]; sb[h] = stl[64 + r0 + (lane & 15)];
      va[h] = *(LAS const f32x4*)(stl + r0 + 4 * g); vb[h] = *(LAS const f32x4*)(stl + 64 + r0 + 4 * g);
    }
#pragma unroll
    for (int h = 0; h < 2; ++h) {
      const u32x4 kf = pack8(acc[2][2 * u + h] * sa[h] + c4[2] * sb[h], acc[3][2 * u + h] * sa[h] + c4[3] * sb[h]);
      const u32x4 vf = pack8(acc[4 + h][2 * u] * va[0] + vb[0] * cv[h], acc[4 + h][2 * u + 1] * va[1] + vb[1] * cv[h]);
      if (u == 1) { st16(skv, lane * 16, wid * 4096 + h * 1024, kf); st16(skv, lane * 16, wid * 4096 + 2048 + h * 1024, vf); }
      *(LAS u32x4*)(kv + h * 1024) = kf; *(LAS u32x4*)(kv + 2048 + h * 1024) = vf;
    }
    asm volatile("" ::: "memory");
#pragma unroll
    for (int h = 0; h < 2; ++h) {
      const float qa = sa[h] * qs, qb = sb[h] * qs;
      const bf16x8 qf = __builtin_bit_cast(bf16x8, pack8(acc[0][2 * u + h] * qa + c4[0] * qb, acc[1][2 * u + h] * qa + c4[1] * qb));
      if (u == 1) Q[h] = qf; else Qn[h] = qf;
    }
  }
  LAS unsigned* cnt = (LAS unsigned*)(lds + 3 * G2_STAGE);
  if (wid == 0 && lane == 0) *cnt = 0u;
  const srd_t sro = mk_srd(ws + WS_O, S_ * D_ * 2);
  LAS const char* kv = lds + (8 + wid) * 4096 + lane * 16;
  unsigned ooff = (unsigned)((sc * 32 + 16 + 2 * wid) * 32 + head) * 1024u;
#pragma nounroll
  for (int u = 1; u >= 0; --u) {
    LGKM0_BAR();
    if (u) signal_wave(cnt, ws, FL_KV + head * 4 + sc, tag, lane);
    if (sc == 0 && u == 0) attn_unit<true>(kv, Q, sro, ooff, 8 - wid, lane); else attn_unit<false>(kv, Q, sro, ooff, 0, lane);
    Q[0] = Qn[0]; Q[1] = Qn[1]; kv -= 32768; ooff -= 16u * 32768u;
  }
  LGKM0_BAR();
}

struct F { static constexpr bool value = false; };
struct T { static constexpr bool value = true; };
template <int GRP, int ROLE>
DI void gemm2_tile(LAS char* lds, const u16* x, const u16* b2, u16* out, char* ws, u64 tag, int st_, int dt, int w, int lane, int sig) {
  constexpr int N1 = ROLE ? G2_N1 : G2_NA, NS = ROLE ? G2_NB : G2_NA;
  const int wr = w >> 1, wc = w & 1, g = lane >> 4, q = (lane & 15) >> 2, p = lane & 3;
  const int r = 4 * (g & 1) + q, sx = (r >> 1) & 3;
  const int srow = st_ * 128 + wc * 64 + GRP * 32 + (lane & 15);
  f32x4 acc[4][4];
#pragma unroll
  for (int i = 0; i < 4; ++i)
#pragma unroll
    for (int jj = 0; jj < 4; ++jj) acc[i][jj] = splat(0.f);
  if (ROLE == 1) LGKM0_BAR();
  const int tile = st_ * 8 + dt;
  unsigned short bv[2];
  int s = GRP, slot = (ROLE ? 2 : 4) - GRP;
  int woff[4], aoff;
  auto step = [&](auto part) {
    constexpr bool P2 = decltype(part)::value;
    LAS const char* st = lds + slot * G2_STAGE;
    bf16x8 A[2][4], W[2][4];
#pragma unroll
    for (int f = 0; f < 8; ++f) {
      W[f >> 2][f & 3] = lds_tr(st + woff[f & 3] + (f >> 2) * 4096, 2048);
      A[f >> 2][f & 3] = lds_frag(st + aoff + (f & 3) * 2048 + (f >> 2) * 1024);
      asm volatile("" ::: "memory");
    }
    if (GRP == 1) LGKM0_BAR();
    SBAR();
    __builtin_amdgcn_s_setprio(GRP ? PRIO1 : PRIO0);
#pragma unroll
    for (int ks = 0; ks < 2; ++ks)
#pragma unroll
      for (int i = 0; i < 4; ++i)
#pragma unroll
        for (int jj = 0; jj < 4; ++jj) acc[i][jj] = P2 ? MFMA(W[ks][i], A[ks][jj], acc[i][jj]) : MFMA(A[ks][i], W[ks][jj], acc[i][jj]);
    __builtin_amdgcn_s_setprio(0);
    SBAR();
    if (GRP == 0) LGKM0_BAR();
    s += 2; slot -= 2; if (slot < 0) slot += G2_R;
  };
#pragma unroll
  for (int i = 0; i < 4; ++i) woff[i] = 16384 + (wc * 8 + (g >> 1)) * 1024 + r * 128 + ((i ^ sx) * 32) + p * 8;
  aoff = wr * 8192 + lane * 16;
  if (ROLE == 1 && GRP == 0) {
    _Pragma("nounroll") while (s < N1 - 10) step(F{});
    u64 seen = 0;
    if (w == 0) seen = __hip_atomic_load(flagp(ws, FL_O + (st_ >> 2) * 32 + (lane & 31)), __ATOMIC_RELAXED, __HIP_MEMORY_SCOPE_AGENT);
    _Pragma("nounroll") while (s < N1 - 6) step(F{});
    asm volatile("" : "+v"(seen));
    if (w == 0 && !__all(seen == tag)) wait_words(ws, FL_O + (st_ >> 2) * 32, 32, tag, lane);
  }
  if (ROLE == 0) {
    u64 seen = tag;
    if (GRP == 0 && w == 0) seen = __hip_atomic_load(flagp(ws, FL_U + dt * 32 + 16 + (lane & 15)), __ATOMIC_RELAXED, __HIP_MEMORY_SCOPE_AGENT);
    _Pragma("nounroll") while (s < 2) step(F{});
    asm volatile("s_waitcnt vmcnt(0)" : "+v"(seen)::"memory");
    if (GRP == 0 && !__all(seen == tag)) wait_words(ws, FL_U + dt * 32 + 16, 16, tag, lane);
    _Pragma("nounroll") while (s < 4) step(F{});
    if (GRP == 0 && w == 0 && lane == 0) post(ws, sig, tag);
  }
  _Pragma("nounroll") while (s < N1) step(F{});
  if (ROLE == 1) {
#pragma unroll
    for (int i = 0; i < 4; ++i) woff[i] = 16384 + (wr * 8 + (g >> 1)) * 1024 + r * 128 + (((4 * (i >> 1) + p) ^ (sx << 1)) * 16) + (i & 1) * 8;
    aoff = wc * 8192 + lane * 16;
    bv[0] = b2[srow]; bv[1] = b2[srow + 16];
    _Pragma("nounroll") while (s < NS) step(T{});
  }
  const srd_t srp = mk_srd(ws + WS_P + (size_t)tile * 32768, 32768);
  const unsigned pofs = (GRP * 4 + w) * 4096 + lane * 16;
  LAS char* xo = lds + G2Load<ROLE>::slot_of(NS - 2 + GRP) * G2_STAGE + w * 8192 + lane * 16;
  LAS const char* xi = lds + G2Load<ROLE>::slot_of(NS - 1 - GRP) * G2_STAGE + w * 8192 + lane * 16;
  LAS char* xt = lds + G2Load<ROLE>::XS * G2_STAGE + (GRP * 4 + w) * 4096;
  auto send = [&]() {
#pragma unroll
    for (int i = 0; i < 4; ++i)
#pragma unroll
      for (int jj = 0; jj < 2; ++jj) *(LAS f32x4*)(xo + (i * 2 + jj) * 1024) = acc[i][2 * (1 - GRP) + jj];
  };
  auto recv = [&]() {
#pragma unroll
    for (int i = 0; i < 4; ++i)
#pragma unroll
      for (int jj = 0; jj < 2; ++jj) acc[i][2 * GRP + jj] += *(LAS const f32x4*)(xi + (i * 2 + jj) * 1024);
  };
  send();
  LGKM0_BAR();
  recv();
  auto xaddr = [&](int jj, int m) { return (LAS u32x4*)(xt + (jj * 16 + (lane & 15)) * 128 + (((m * 4 + g) ^ (lane & 7)) * 16)); };
  auto add8 = [&](int jj, int m, u32x4 v, float c) {
    const f32x2 p0 = unpk(v[0]) + c, p1 = unpk(v[1]) + c, p2 = unpk(v[2]) + c, p3 = unpk(v[3]) + c;
    acc[2 * m][2 * GRP + jj] += (f32x4){p0.x, p0.y, p1.x, p1.y}; acc[2 * m + 1][2 * GRP + jj] += (f32x4){p2.x, p2.y, p3.x, p3.y};
  };
  if (ROLE == 1) {
#pragma unroll
    for (int k = 0; k < 4; ++k) add8(k >> 1, k & 1, *xaddr(k >> 1, k & 1), bf2f(bv[k >> 1]));
#pragma unroll
    for (int k = 0; k < 4; ++k) asm volatile("" : "+v"(acc[k][2 * GRP]), "+v"(acc[k][2 * GRP + 1]));
    BAR();
  }
#pragma unroll
  for (int jj = 0; jj < 2; ++jj)
#pragma unroll
    for (int m = 0; m < 2; ++m) {
      if (ROLE == 1) add8(jj, m, *(LAS const u32x4*)(lds + G2Load<ROLE>::PS * G2_STAGE + pofs + (jj * 2 + m) * 1024), 0.f);
      const u32x4 r8 = pack8(acc[2 * m][2 * GRP + jj], acc[2 * m + 1][2 * GRP + jj]);
      if (ROLE == 0) __builtin_amdgcn_raw_buffer_store_b128(r8, srp, pofs + (jj * 2 + m) * 1024, 0, 16); else *xaddr(jj, m) = r8;
    }
  if (ROLE == 1) {
    asm volatile("s_waitcnt lgkmcnt(0)" ::: "memory");
    const unsigned oo = ((st_ * 128 + wc * 64 + GRP * 32 + (lane >> 3)) * D_ + dt * 128 + wr * 64) * 2 + (((lane & 7) ^ (lane >> 3)) * 16);
#pragma unroll
    for (int k = 0; k < 4; ++k) __builtin_amdgcn_raw_buffer_store_b128(*(LAS const u32x4*)(xt + k * 1024 + lane * 16), mk_srd(out, S_ * D_ * 2), oo + k * 16384, 0, 16);
  }
  if (ROLE == 0) {
    WAIT_BAR(0);
    if (GRP == 0 && w == 0 && lane == 0) post(ws, FL_P + tile, tag);
  }
}
__global__ __launch_bounds__(768, 3) void k_main(const u16* __restrict__ x, Weights W, const u16* __restrict__ b1, const u16* __restrict__ b2, u16* __restrict__ out, char* __restrict__ ws) {
  extern __shared__ __attribute__((aligned(16))) char smem[];
  LAS char* lds = (LAS char*)smem;
  const unsigned lds0 = (unsigned)(uintptr_t)lds;
  const int wid = __builtin_amdgcn_readfirstlane(threadIdx.x >> 6), lane = threadIdx.x & 63, w = wid & 3;
  const int xc = blockIdx.x & 7, idx = blockIdx.x >> 3;
  const bool isM = idx < 16;
  const int t = idx & 15, j = xc * 16 + t;
  const int st2 = (xc >> 1) * 4 + (t & 3), dt = (xc & 1) * 4 + (t >> 2);
  const int sc = (xc & 1) * 2 + (t >> 3), head = (xc >> 1) * 8 + (t & 7);
  u32x4 v[4]; u64 tag = 0;
  if (wid < 8) {
    if (isM) {
      const u16* xr = x + (size_t)(8 * j + wid) * D_ + lane * 8;
#pragma unroll
      for (int k = 0; k < 4; ++k) v[k] = *(const u32x4*)(xr + (k >> 1) * (1024 * D_) + (k & 1) * 512);
    }
    tag = __hip_atomic_load(flagp(ws, 0), __ATOMIC_RELAXED, __HIP_MEMORY_SCOPE_AGENT);
  }
  const int ctr = isM ? CTR : QCTR;
  if (threadIdx.x < 16) ((LAS unsigned*)(lds + ctr))[threadIdx.x] = 0u;
  LGKM0_BAR();
  if (wid < 8) {
    tag = uni64(tag);
    tag += 0x9E3779B97F4A7C15ull; tag = (tag ^ (tag >> 30)) * 0xBF58476D1CE4E5B9ull; tag = (tag ^ (tag >> 27)) * 0x94D049BB133111EBull; tag ^= tag >> 31;
    if (threadIdx.x == 0) *(LAS u64*)(lds + ctr + 32) = tag;
    if (isM) {
      ln_phase(lds + LN_SC, (LAS unsigned*)(lds + CTR), v, ws, tag, j, wid, lane);
      if (wid == 0) wait_words(ws, FL_LN, 64, tag, lane);
    } else signal_wave((LAS unsigned*)(lds + QCTR) + 1, ws, FL_HI + j, tag, lane);
  }
  if (isM) {
    if (wid < 4) { b1_tile<0>(lds, b1, ws, tag, dt, st2, w, lane, j == 0); gemm2_tile<0, 1>(lds, x, b2, out, ws, tag, st2, dt, w, lane, 0); }
    else if (wid < 8) { b1_tile<1>(lds, b1, ws, tag, dt, st2, w, lane, false); gemm2_tile<1, 1>(lds, x, b2, out, ws, tag, st2, dt, w, lane, 0); }
    else m_loader(lds, lds0, x, W, ws, dt, st2, st2, w, lane, j);
  } else {
    if (wid < 8) {
      q_tile(lds, ws, tag, sc, head, wid, lane);
      if (wid < 4) gemm2_tile<0, 0>(lds, x, b2, out, ws, tag, st2, dt, w, lane, FL_O + sc * 32 + head); else gemm2_tile<1, 0>(lds, x, b2, out, ws, tag, st2, dt, w, lane, 0);
    } else q_loader(lds, lds0, x, W, ws, sc, head, st2, dt, w, lane, j);
  }
}

extern "C" void kernel_launch(void* const* d_in, const int* in_sizes, int n_in, void* d_out, int out_size, void* d_ws, size_t ws_size, hipStream_t stream) {
  const u16* x = (const u16*)d_in[0];
  Weights W; W.wqkv = (const u16*)d_in[1]; W.w1 = (const u16*)d_in[4]; W.w2 = (const u16*)d_in[6]; W.wo = (const u16*)d_in[2];
  char* ws = (char*)d_ws;
  (void)hipFuncSetAttribute((const void*)k_main, hipFuncAttributeMaxDynamicSharedMemorySize, K2_LDS);
  k_main<<<256, 768, K2_LDS, stream>>>(x, W, (const u16*)d_in[5], (const u16*)d_in[7], (u16*)d_out, ws);
}
```
